```python
import torch

def _softmax(x, axis=-1):
    return torch.softmax(x, dim=axis)
def _gelu(x):
    return torch.nn.functional.gelu(x, approximate="tanh")
def _silu(x):
    return torch.nn.functional.silu(x)
def _sigmoid(x):
    return torch.sigmoid(x)
def _layernorm(x, axis=-1, eps=1e-5):
    m = x.mean(dim=axis, keepdim=True)
    v = x.var(dim=axis, keepdim=True, unbiased=False)
    return (x - m) / torch.sqrt(v + eps)
def _scatter_add(tgt, idx, src, axis=0):
    return tgt.clone().index_add_(axis, idx, src)
def _segment_sum(src, seg, n):
    out = torch.zeros((n,) + src.shape[1:], dtype=src.dtype)
    return out.index_add_(0, seg, src)
def _pad(x, pw):
    flat = tuple(v for pair in reversed(pw) for v in pair)
    return torch.nn.functional.pad(x, flat)

kernel_name = 'recurrent_scan__spectral_gfnet__dense_activation_squared_relu__prompt'

def setup_inputs():

    _w = (lambda a: (lambda t: t.to(torch.bfloat16) if t.is_floating_point() else t)(torch.as_tensor(a)))
    def _A_setup():
        B, S, D = 1, 2048, 576
        Df = D // 2 + 1
        return {
            "x": _w((lambda *s: torch.randn(*s, dtype=torch.bfloat16) * 0.05)(B, S, D)),
            "w_r": _w((lambda *s: torch.randn(*s, dtype=torch.bfloat16) * 0.05)(S, Df) * 0.02),
            "w_i": _w((lambda *s: torch.randn(*s, dtype=torch.bfloat16) * 0.05)(S, Df) * 0.02),
        }
    _A_in = _A_setup()

    def _B_setup():
        B, S, D, F = 1, 2048, 576, 4608
        return {
            "x": _w((lambda *s: torch.randn(*s, dtype=torch.bfloat16) * 0.05)(B, S, D)),
            "w1": _w((lambda *s: torch.randn(*s, dtype=torch.bfloat16) * 0.05)(D, F)),
            "b1": _w((lambda *s: torch.randn(*s, dtype=torch.bfloat16) * 0.4)(F)),
            "w2": _w((lambda *s: torch.randn(*s, dtype=torch.bfloat16) * 0.05)(F, D)),
            "b2": _w((lambda *s: torch.randn(*s, dtype=torch.bfloat16) * 3.2)(D)),
        }
    _B_in = _B_setup()

    return {
        "A_x": _A_in["x"],
        "A_w_r": _A_in["w_r"],
        "A_w_i": _A_in["w_i"],
        "B_x": _B_in["x"],
        "B_w1": _B_in["w1"],
        "B_b1": _B_in["b1"],
        "B_w2": _B_in["w2"],
        "B_b2": _B_in["b2"],
    }

def reference(A_x, A_w_r, A_w_i, B_x, B_w1, B_b1, B_w2, B_b2):
    def _A_ref(x, w_r, w_i):
        B, S, D = x.shape
        X = (lambda x, axes=(-2,-1): torch.fft.rfft2(x.float(), dim=axes))(x, axes=(-2, -1))
        W = w_r + 1j * w_i
        X = X * W
        y = (lambda x, s, axes=(-2,-1): torch.fft.irfft2(x, s=s, dim=axes).to(torch.bfloat16))(X, (S, D), axes=(-2, -1))
        return y

    def _B_ref(x, w1, b1, w2, b2):
        h = torch.einsum("bsd,df->bsf", x, w1) + b1
        zero = torch.zeros_like(h)
        r = torch.where(h > zero, h, zero)
        h = r * r
        out = torch.einsum("bsf,fd->bsd", h, w2) + b2
        return out

    x = A_x
    xn = _layernorm(x, axis=-1)
    s = _A_ref(xn, A_w_r, A_w_i)
    g = _silu(_B_ref(xn, B_w1, B_b1, B_w2, B_b2))
    return x + s * g
```

```cpp
#include <hip/hip_runtime.h>
#include <utility>

typedef __attribute__((ext_vector_type(8))) __bf16 bf16x8;
typedef __attribute__((ext_vector_type(4))) __bf16 bf16x4;
typedef __attribute__((ext_vector_type(2))) __bf16 bf16x2;
typedef __attribute__((ext_vector_type(8))) _Float16 f16x8;
typedef __attribute__((ext_vector_type(4))) _Float16 f16x4;
typedef __attribute__((ext_vector_type(2))) _Float16 f16x2;
typedef __attribute__((ext_vector_type(2))) float f32x2;
typedef __attribute__((ext_vector_type(4))) float f32x4;
typedef __attribute__((ext_vector_type(4))) unsigned u32x4;
typedef __attribute__((ext_vector_type(2))) unsigned u32x2;
#define LAS __attribute__((address_space(3)))
#define DI __device__ __forceinline__

namespace ct {
constexpr double PI = 3.14159265358979323846264338327950288;
constexpr double cos_r(double x) { double t = 1, s = 1; for (int i = 1; i <= 14; ++i) { t *= -x * x / ((2 * i - 1) * (2 * i)); s += t; } return s; }
constexpr double cosn(int i, int n) {
  i %= n; if (2 * i > n) i = n - i; if (4 * i == n) return 0.0;
  if (4 * i > n) return -cos_r(PI * (n - 2 * i) / n);
  return cos_r(2 * PI * i / n); }
constexpr unsigned short f2bf(double d) { float f = (float)d; unsigned u = __builtin_bit_cast(unsigned, f); u += 0x7fffu + ((u >> 16) & 1u); return (unsigned short)(u >> 16); }
constexpr unsigned short f2h(double d) { return __builtin_bit_cast(unsigned short, (_Float16)d); }
constexpr float bf2f(unsigned short b) { return __builtin_bit_cast(float, (unsigned)b << 16); }
struct Lut576 { unsigned short v[576]; };
constexpr Lut576 mk576() { Lut576 l{}; for (int i = 0; i < 576; ++i) l.v[i] = f2bf(cosn(i, 576)); return l; }
constexpr Lut576 L576 = mk576();
struct Lut2048 { unsigned short h[2048], b[2048], bh[2048]; };
constexpr Lut2048 mk2048() { Lut2048 l{}; for (int i = 0; i < 2048; ++i) { double c = cosn(i, 2048); l.h[i] = f2h(c); l.b[i] = f2bf(c); l.bh[i] = f2bf(0.5 * c); } return l; }
constexpr Lut2048 L2048 = mk2048();
constexpr unsigned short cval(int d, int kk) { return kk == 1 ? L576.v[(d & 1) * 288] : L576.v[((kk >> 1) * d + (kk & 1) * 144) % 576]; }
struct Unit { unsigned short v[18 * 512]; };
constexpr Unit mkunit(int u, bool inv) { Unit r{}; for (int ks = 0; ks < 18; ++ks) for (int l = 0; l < 64; ++l) for (int i = 0; i < 8; ++i) {
  int a = 32 * ks + 8 * (l >> 4) + i, b = 16 * u + (l & 15); r.v[(ks * 64 + l) * 8 + i] = inv ? cval(b, a) : cval(a, b); } return r; }
template <int U, bool INV> struct UH { static constexpr Unit v = mkunit(U, INV); };
constexpr int pk(int g, int i) { return 16 * (i >> 2) + 4 * g + (i & 3); }
struct MQ { unsigned short f[16 * 512]; unsigned short i[16 * 512]; };
constexpr MQ mkmq(int q) { MQ r{};
  for (int cs = 0; cs < 2; ++cs) for (int mt = 0; mt < 4; ++mt) for (int jk = 0; jk < 2; ++jk) for (int l = 0; l < 64; ++l) for (int i = 0; i < 8; ++i) {
    int m = 16 * mt + (l & 15), j = 32 * jk + pk(l >> 4, i);
    r.f[(((mt * 2 + cs) * 2 + jk) * 64 + l) * 8 + i] = L2048.h[(j * (q + 32 * m) + cs * 1536) & 2047]; }
  bool half = (q == 0 || q == 16);
  for (int jt = 0; jt < 4; ++jt) for (int mt = 0; mt < 4; ++mt) for (int l = 0; l < 64; ++l) for (int i = 0; i < 8; ++i) {
    int m = 16 * mt + 4 * (l >> 4) + (i & 3), j = 16 * jt + (l & 15); int idx = (j * (q + 32 * m) + (i >> 2) * 1536) & 2047;
    r.i[((jt * 4 + mt) * 64 + l) * 8 + i] = half ? L2048.bh[idx] : L2048.b[idx]; }
  return r; }
template <int Q> struct MH { static constexpr MQ v = mkmq(Q); };
constexpr int hidx(int rho, int r) { return rho <= 16 ? (64 * rho * r) & 2047 : (64 * (rho - 16) * r + 1536) & 2047; }
struct HS { unsigned short f[2 * 512]; unsigned short i[2 * 512]; float cs[576]; };
constexpr HS mkhs() { HS r{};
  for (int t = 0; t < 2; ++t) for (int l = 0; l < 64; ++l) for (int i = 0; i < 8; ++i) {
    r.f[(t * 64 + l) * 8 + i] = L2048.h[hidx(16 * t + (l & 15), pk(l >> 4, i))];
    r.i[(t * 64 + l) * 8 + i] = L2048.b[hidx(pk(l >> 4, i), 16 * t + (l & 15))]; }
  for (int kk = 0; kk < 576; ++kk) r.cs[kk] = 0.f;
  const int ks[5] = {0, 128, 256, 384, 512};
  for (int a = 0; a < 5; ++a) { double s = 0; for (int d = 0; d < 576; ++d) s += bf2f(cval(d, ks[a])); r.cs[ks[a]] = (float)s; }
  return r; }
struct Tab { Unit cf[36]; Unit ci[36]; MQ mq[17]; HS hs; };
template <int... I, int... Q> constexpr Tab mktab(std::integer_sequence<int, I...>, std::integer_sequence<int, Q...>) {
  return Tab{{UH<I, false>::v...}, {UH<I, true>::v...}, {MH<Q>::v...}, mkhs()}; }
constexpr unsigned OFF_CF = 0, OFF_CI = 36 * 18432, OFF_MQ = 72 * 18432, OFF_HS = OFF_MQ + 17 * 32768, TAB_BYTES = OFF_HS + 4096 + 2304;
static_assert(sizeof(Tab) == TAB_BYTES, "tab");
}
#if defined(__HIP_DEVICE_COMPILE__)
__device__ __attribute__((aligned(1024))) const ct::Tab g_tab = ct::mktab(std::make_integer_sequence<int, 36>{}, std::make_integer_sequence<int, 17>{});
#define TABP ((const char*)&g_tab)
#else
#define TABP ((const char*)nullptr)
#endif

constexpr unsigned WS_STAT = 0;
constexpr unsigned WS_CS   = 16384;
constexpr unsigned WS_E    = 65536;
constexpr unsigned WS_G    = WS_E + 2359296;
constexpr unsigned WS_H    = WS_G + 2359296;
constexpr unsigned WS_P    = WS_H + 18874368;
constexpr unsigned WS_END  = WS_P + 4 * 2359296;

DI u32x4 mk_rsrc(const void* p, unsigned bytes) { unsigned long long a = (unsigned long long)p; return (u32x4){(unsigned)a, (unsigned)(a >> 32) & 0xffffu, bytes, 0x00020000u}; }
DI void dma16(unsigned lds, unsigned voff, u32x4 rsrc, unsigned soff) {
  asm volatile("s_mov_b32 m0, %0\n\ts_nop 0\n\tbuffer_load_dwordx4 %1, %2, %3 offen lds" :: "s"(lds), "v"(voff), "s"(rsrc), "s"(soff) : "memory");
}
#define WAITV(n) asm volatile("s_waitcnt vmcnt(%0)" :: "n"(n) : "memory")
#define BARRIER() asm volatile("s_waitcnt lgkmcnt(0)\n\ts_barrier" ::: "memory")
#define SCHED() __builtin_amdgcn_sched_barrier(0)
DI bf16x8 lds_b128(unsigned a) { return *(LAS bf16x8*)a; }
DI bf16x8 lds_tr(unsigned a0, unsigned a1) {
  bf16x4 lo = __builtin_amdgcn_ds_read_tr16_b64_v4bf16((LAS bf16x4*)a0), hi = __builtin_amdgcn_ds_read_tr16_b64_v4bf16((LAS bf16x4*)a1);
  return __builtin_shufflevector(lo, hi, 0, 1, 2, 3, 4, 5, 6, 7); }
DI f32x4 mfma_bf(bf16x8 a, bf16x8 b, f32x4 c) { return __builtin_amdgcn_mfma_f32_16x16x32_bf16(a, b, c, 0, 0, 0); }
DI f32x4 mfma_h(f16x8 a, f16x8 b, f32x4 c) { return __builtin_amdgcn_mfma_f32_16x16x32_f16(a, b, c, 0, 0, 0); }
DI unsigned pk_bf(float a, float b) { return __builtin_bit_cast(unsigned, __builtin_convertvector((f32x2){a, b}, bf16x2)); }
DI unsigned pk_h(float a, float b) { return __builtin_bit_cast(unsigned, __builtin_convertvector((f32x2){a, b}, f16x2)); }
DI float bf_lo(unsigned u) { return __uint_as_float(u << 16); }
DI float bf_hi(unsigned u) { return __uint_as_float(u & 0xffff0000u); }
DI float rbf(float x) { return bf_lo(pk_bf(x, 0.f)); }

__global__ __launch_bounds__(256) void k0(const unsigned short* x, const unsigned short* w1, char* ws) {
  int b = blockIdx.x, tid = threadIdx.x, lane = tid & 63, w = tid >> 6;
  if (b < 512) {
    int t = b * 4 + w, s = (t >> 5) + 64 * (t & 31);
    float v[9], sum = 0;
    for (int i = 0; i < 9; ++i) { v[i] = bf_lo(x[s * 576 + lane + 64 * i]); sum += v[i]; }
    for (int o = 32; o; o >>= 1) sum += __shfl_xor(sum, o);
    float mean = sum * (1.f / 576.f), q = 0;
    for (int i = 0; i < 9; ++i) q += (v[i] - mean) * (v[i] - mean);
    for (int o = 32; o; o >>= 1) q += __shfl_xor(q, o);
    float var = rbf(q * (1.f / 576.f)), sd = rbf(sqrtf(rbf(var + 1e-5f)));
    if (lane == 0) *(f32x2*)(ws + WS_STAT + t * 8) = (f32x2){rbf(mean), 1.f / sd};
  } else {
    int f = (b - 512) * 256 + tid; float s = 0;
    for (int k = 0; k < 576; ++k) s += bf_lo(w1[k * 4608 + f]);
    *(float*)(ws + WS_CS + f * 4) = s;
  }
}

constexpr int K1_A = 0, K1_B = 49152, K1_BS = 21504, K1_LDS = K1_B + 5 * K1_BS;
struct Frag1 { bf16x8 a[4], b[5], e; };

template <bool X> DI void k1_body(const unsigned short* x, const unsigned short* w1, const unsigned short* b1, char* ws, int p, int c, int w, int lane) {
  const int g = lane >> 4, n = lane & 15;
  const int mh = (w >> 1) & 1, nq = (w & 1) + (X ? 0 : 2);
  const bool extra = (c & 3) == 0;
  const u32x4 rx = mk_rsrc(x, 2048 * 1152), rw = mk_rsrc(w1, 576 * 9216), rt = mk_rsrc(TABP, ct::TAB_BYTES);
  unsigned voffA; { int i = 8 * w + (lane >> 3); int s = 4 * p + (i >> 5) + 64 * (i & 31); voffA = s * 1152 + (((lane & 7) ^ ((i >> 1) & 7)) << 4); }
  auto boff = [&](int ins) { int ci = ins * 64 + lane, row = ci / 36, pos = ci - row * 36; return (unsigned)(row * 9216 + c * 576 + ((pos ^ (((row >> 3) & 1) << 1)) << 4)); };
  const unsigned voffB0 = boff(w), voffB1 = boff(w + 8);
  const bool third = X || (w == 4 && extra);
  const bool third_w = w < 2;
  const unsigned voff3 = third_w ? boff(w + 16) : (unsigned)(ct::OFF_CF + (w == 4 ? 32 + (c >> 2) : 2 * c + (w - 2)) * 18432 + lane * 16);
  u32x4 r3 = third_w ? rw : rt;
  const unsigned step3 = third_w ? 32 * 9216 : 1024;
  auto issueA = [&](int a, int hh) { dma16(K1_A + (a % 3) * 16384 + hh * 8192 + w * 1024, voffA, rx, a * 128 + hh * 2304); };
  auto issueB = [&](int st, int slot) { unsigned lb = K1_B + slot * K1_BS + w * 1024;
    dma16(lb, voffB0, rw, st * (32 * 9216)); dma16(lb + 8192, voffB1, rw, st * (32 * 9216));
    if (third) dma16(lb + 16384, voff3, r3, st * step3); };
  issueA(0, 0); issueA(0, 1); issueB(0, 0); issueB(1, 1); issueA(1, 0); issueA(1, 1); issueB(2, 2); issueB(3, 3); issueA(2, 0); issueA(2, 1); issueB(4, 4);
  const int u0 = X ? 5 * nq : 10 + 4 * (nq - 2);
  const unsigned rdA = K1_A + (64 * mh + n) * 128 + ((g ^ ((n >> 1) & 7)) << 4);
  const unsigned trl = K1_B + (8 * g + (n >> 2)) * 576 + 8 * (n & 3) + u0 * 32;
  const unsigned trE = trl + ((u0 & 1) ? -32 : 32) * (g & 1), trO = trl - ((u0 & 1) ? -32 : 32) * (g & 1);
  const unsigned rdD = K1_B + 18432 + (X ? 2048 : (nq - 2) * 1024) + lane * 16;
  constexpr int NB = X ? 3 : 2;
  f32x4 acc[4][5], acce[2];
#pragma unroll
  for (int m = 0; m < 4; ++m)
#pragma unroll
    for (int u = 0; u < 5; ++u) acc[m][u] = (f32x4){0, 0, 0, 0};
  acce[0] = acce[1] = (f32x4){0, 0, 0, 0};
  auto rd = [&](Frag1& F, int st, int slotA, int slotB) {
    unsigned a = (rdA + slotA * 16384) ^ ((st & 1) << 6), so = slotB * K1_BS;
#pragma unroll
    for (int m = 0; m < 4; ++m) F.a[m] = lds_b128(a + m * 2048);
#pragma unroll
    for (int u = 0; u < (X ? 5 : 4); ++u) { unsigned b = ((u & 1) ? trO : trE) + so + u * 32; F.b[u] = lds_tr(b, b + 4 * 576); }
    if (!X) F.b[4] = lds_b128(rdD + so);
    if (X) F.e = lds_b128(rdD + so);
  };
  auto mm = [&](const Frag1& F) {
#pragma unroll
    for (int u = 0; u < 5; ++u)
#pragma unroll
      for (int m = 0; m < 4; ++m) acc[m][u] = (X || u < 4) ? mfma_bf(F.b[u], F.a[m], acc[m][u]) : mfma_bf(F.a[m], F.b[u], acc[m][u]);
    if (X && extra) {
      if (nq == 0) { acce[0] = mfma_bf(F.a[0], F.e, acce[0]); acce[1] = mfma_bf(F.a[1], F.e, acce[1]); }
      else         { acce[0] = mfma_bf(F.a[2], F.e, acce[0]); acce[1] = mfma_bf(F.a[3], F.e, acce[1]); }
    }
  };
  Frag1 F0, F1;
  WAITV(4 * NB + 4); BARRIER();
  rd(F0, 0, 0, 0);
  int sB = 0, sA = 0;
  for (int i = 0; i < 18; i += 2) {
    if (i <= 13) WAITV(3 * NB + 2); else WAITV(0);
    BARRIER(); SCHED();
    if (i <= 12) { if (i) issueA((i - 1) / 2 + 3, 1); issueB(i + 5, sB); }
    int sB1 = sB == 4 ? 0 : sB + 1;
    rd(F1, 1, sA, sB1);
    mm(F0); SCHED();
    if (i + 1 <= 13) WAITV(3 * NB + 2); else WAITV(0);
    BARRIER(); SCHED();
    if (i + 1 <= 12) { issueA(i / 2 + 3, 0); issueB(i + 6, sB1); }
    sB = sB1 == 4 ? 0 : sB1 + 1; sA = sA == 2 ? 0 : sA + 1;
    if (i < 16) rd(F0, 0, sA, sB);
    mm(F1); SCHED();
  }
  const f32x2* stat = (const f32x2*)(ws + WS_STAT) + 128 * p + 64 * mh;
  const float* cs = (const float*)(ws + WS_CS) + 288 * c;
  __amdgpu_buffer_rsrc_t rws = __builtin_amdgcn_make_buffer_rsrc(ws, 0, WS_END, 0x00020000);
  auto dft_out = [&](f32x4 t0, f32x4 t1, int row0, int unit) -> void {
    float csu = *(const float*)(TABP + ct::OFF_HS + 4096 + (unit * 16 + n) * 4);
    f32x4 s0a = *(const f32x4*)(stat + row0 + 4 * g), s0b = *(const f32x4*)(stat + row0 + 4 * g + 2), s1a = *(const f32x4*)(stat + row0 + 16 + 4 * g), s1b = *(const f32x4*)(stat + row0 + 18 + 4 * g);
    u32x4 pk = {pk_h((t0[0] - s0a[0] * csu) * s0a[1], (t0[1] - s0a[2] * csu) * s0a[3]), pk_h((t0[2] - s0b[0] * csu) * s0b[1], (t0[3] - s0b[2] * csu) * s0b[3]),
                pk_h((t1[0] - s1a[0] * csu) * s1a[1], (t1[1] - s1a[2] * csu) * s1a[3]), pk_h((t1[2] - s1b[0] * csu) * s1b[1], (t1[3] - s1b[2] * csu) * s1b[3])};
    int j = 4 * p + 2 * mh + (row0 >> 5);
#pragma unroll
    for (int rt2 = 0; rt2 < 2; ++rt2) {
      f16x8 hf = *(const f16x8*)(TABP + ct::OFF_HS + rt2 * 1024 + lane * 16);
      f32x4 e = mfma_h(__builtin_bit_cast(f16x8, pk), hf, (f32x4){0, 0, 0, 0});
      u32x2 o = {pk_h(e[0], e[1]), pk_h(e[2], e[3])};
      __builtin_amdgcn_raw_buffer_store_b64(o, rws, WS_E + (((16 * rt2 + n) * 36 + unit) * 64 + j) * 32 + g * 8, 0, 16);
    }
  };
  if (!X) { dft_out(acc[0][4], acc[1][4], 0, 2 * c + nq - 2); dft_out(acc[2][4], acc[3][4], 32, 2 * c + nq - 2); }
  if (X && extra) dft_out(acce[0], acce[1], 32 * nq, 32 + (c >> 2));
  constexpr int NU = X ? 5 : 4;
  f32x4 csv[NU], bv[NU];
#pragma unroll
  for (int u = 0; u < NU; ++u) { csv[u] = *(const f32x4*)(cs + (u0 + u) * 16 + 4 * g);
    u32x2 bb = *(const u32x2*)(b1 + 288 * c + (u0 + u) * 16 + 4 * g); bv[u] = (f32x4){bf_lo(bb[0]), bf_hi(bb[0]), bf_lo(bb[1]), bf_hi(bb[1])}; }
#pragma unroll
  for (int m = 0; m < 4; ++m) {
    f32x2 st = stat[16 * m + n]; float rs = st[1], mr = -st[0] * st[1];
    u32x2 hp[NU];
#pragma unroll
    for (int u = 0; u < NU; ++u) { float v[4];
#pragma unroll
      for (int r = 0; r < 4; ++r) { float t = fmaf(acc[m][u][r], rs, fmaf(mr, csv[u][r], bv[u][r])); t = fmaxf(t, 0.f); v[r] = t * t; }
      hp[u] = (u32x2){pk_bf(v[0], v[1]), pk_bf(v[2], v[3])}; }
    unsigned base = WS_H + ((8 * p + 4 * mh + m) * 144 + 9 * c) * 1024 + lane * 16;
    if (X && nq == 1) {
      __builtin_amdgcn_raw_buffer_store_b64(hp[0], rws, base + 2 * 1024 + 8, 0, 16);
      __builtin_amdgcn_raw_buffer_store_b128((u32x4){hp[1][0], hp[1][1], hp[2][0], hp[2][1]}, rws, base + 3 * 1024, 0, 16);
      __builtin_amdgcn_raw_buffer_store_b128((u32x4){hp[3][0], hp[3][1], hp[NU - 1][0], hp[NU - 1][1]}, rws, base + 4 * 1024, 0, 16);
    } else {
      unsigned b2 = base + (u0 >> 1) * 1024;
      __builtin_amdgcn_raw_buffer_store_b128((u32x4){hp[0][0], hp[0][1], hp[1][0], hp[1][1]}, rws, b2, 0, 16);
      __builtin_amdgcn_raw_buffer_store_b128((u32x4){hp[2][0], hp[2][1], hp[3][0], hp[3][1]}, rws, b2 + 1024, 0, 16);
      if (X) __builtin_amdgcn_raw_buffer_store_b64(hp[NU - 1], rws, b2 + 2048, 0, 16);
    }
  }
}
__global__ __launch_bounds__(512, 2) void k1(const unsigned short* x, const unsigned short* w1, const unsigned short* b1, char* ws) {
  extern __shared__ __attribute__((aligned(1024))) char smem[];
  int b = blockIdx.x, xcd = b & 7, idx = b >> 3;
  int p = (xcd >> 2) * 8 + (idx >> 2), c = (xcd & 3) * 4 + (idx & 3);
  int w = __builtin_amdgcn_readfirstlane(threadIdx.x >> 6), lane = threadIdx.x & 63;
  if (w < 4) k1_body<true>(x, w1, b1, ws, p, c, w, lane); else k1_body<false>(x, w1, b1, ws, p, c, w, lane);
}

__global__ __launch_bounds__(256) void kmid(const unsigned short* wr, const unsigned short* wi, char* ws) {
  __shared__ __attribute__((aligned(1024))) char smem[12288 + 12288];
  int q = blockIdx.x / 12, ub = (blockIdx.x % 12) * 3;
  int w = __builtin_amdgcn_readfirstlane(threadIdx.x >> 6), lane = threadIdx.x & 63, g = lane >> 4, n = lane & 15;
  unsigned lb = (unsigned)(size_t)smem;
  const u32x4 rws = mk_rsrc(ws, WS_END);
  const bool hasS = q != 0 && q != 16;
  for (int i = w; i < 12; i += 4) { int cs = i / 6, pc = i % 6;
    unsigned src = WS_E + ((q + 16 * cs) * 36 + ub) * 2048 + pc * 1024;
    dma16(lb + i * 1024, lane * 16, rws, (cs && !hasS) ? 0xf0000000u : src); }
  const char* mq = TABP + ct::OFF_MQ + q * 32768;
  f16x8 mf[2][2];
#pragma unroll
  for (int cs = 0; cs < 2; ++cs)
#pragma unroll
    for (int jk = 0; jk < 2; ++jk) mf[cs][jk] = *(const f16x8*)(mq + ((w * 2 + cs) * 2 + jk) * 1024 + lane * 16);
  const unsigned short* wsrc = (n & 1) ? wi : wr;
  float wp[3][4], wm[3][4];
#pragma unroll
  for (int r = 0; r < 4; ++r) { int ks = q + 32 * (16 * w + 4 * g + r), nk = (2048 - ks) & 2047;
#pragma unroll
    for (int u = 0; u < 3; ++u) { int k = (ub + u) * 8 + (n >> 1); wp[u][r] = bf_lo(wsrc[ks * 289 + k]); wm[u][r] = bf_lo(wsrc[nk * 289 + k]); } }
  float sx[4] = {0, 0, 0, 0}, dx[4] = {0, 0, 0, 0};
  if (ub == 0) {
#pragma unroll
    for (int r = 0; r < 4; ++r) { int ks = q + 32 * (16 * w + 4 * g + r), nk = (2048 - ks) & 2047;
      sx[r] = bf_lo(wr[ks * 289 + 288]) + bf_lo(wr[nk * 289 + 288]); dx[r] = bf_lo(wi[nk * 289 + 288]) - bf_lo(wi[ks * 289 + 288]); }
  }
  WAITV(0); BARRIER();
  const float SC = 2.0f / (576.f * 2048.f);
  const unsigned tra = lb + (4 * g + (n >> 2)) * 32 + 8 * (n & 3);
#pragma unroll
  for (int u = 0; u < 3; ++u) {
    f32x4 a = {0, 0, 0, 0}, b = {0, 0, 0, 0};
#pragma unroll
    for (int jk = 0; jk < 2; ++jk) {
      unsigned o = tra + u * 2048 + jk * 1024;
      f16x8 ec = __builtin_bit_cast(f16x8, lds_tr(o, o + 512)), es = __builtin_bit_cast(f16x8, lds_tr(o + 6144, o + 6144 + 512));
      a = mfma_h(mf[0][jk], ec, a); a = mfma_h(mf[1][jk], -es, a);
      b = mfma_h(mf[1][jk], ec, b); b = mfma_h(mf[0][jk], es, b);
    }
    float ao[4], bo[4];
    const bool spec = (ub + u == 0) && n < 2;
#pragma unroll
    for (int r = 0; r < 4; ++r) {
      float su = (wp[u][r] + wm[u][r]) * SC, di = (wp[u][r] - wm[u][r]) * SC;
      float su2 = __shfl_xor(su, 1), di2 = __shfl_xor(di, 1);
      bool im = n & 1;
      float Sr = im ? su2 : su, Si = im ? su : -su2, Dr = im ? -di : -di2, Di = im ? di2 : -di;
      if (spec) { Si = 0.f; Di = 0.f; if (im) { Sr = sx[r] * SC; Dr = dx[r] * SC; } Sr *= 0.5f; Dr *= 0.5f; }
      float a2 = __shfl_xor(a[r], 1), b2 = __shfl_xor(b[r], 1);
      ao[r] = a[r] * Sr + a2 * Si - b[r] * Dr - b2 * Di;
      bo[r] = a[r] * Dr + a2 * Di + b[r] * Sr + b2 * Si;
    }
    *(LAS u32x4*)(lb + 12288 + (u * 4 + w) * 1024 + lane * 16) = (u32x4){pk_bf(ao[0], ao[1]), pk_bf(ao[2], ao[3]), pk_bf(bo[0], bo[1]), pk_bf(bo[2], bo[3])};
  }
  bf16x8 mi[4];
#pragma unroll
  for (int mt = 0; mt < 4; ++mt) mi[mt] = *(const bf16x8*)(mq + 16384 + (w * 4 + mt) * 1024 + lane * 16);
  BARRIER();
  __amdgpu_buffer_rsrc_t rw2 = __builtin_amdgcn_make_buffer_rsrc(ws, 0, WS_END, 0x00020000);
#pragma unroll
  for (int u = 0; u < 3; ++u) {
    f32x4 gc = {0, 0, 0, 0}, gs = {0, 0, 0, 0};
#pragma unroll
    for (int mt = 0; mt < 4; ++mt) {
      u32x4 y = *(LAS u32x4*)(lb + 12288 + (u * 4 + mt) * 1024 + lane * 16);
      u32x4 y2 = {y[2], y[3], y[0] ^ 0x80008000u, y[1] ^ 0x80008000u};
      gc = mfma_bf(__builtin_bit_cast(bf16x8, y), mi[mt], gc); gs = mfma_bf(__builtin_bit_cast(bf16x8, y2), mi[mt], gs);
    }
    unsigned o = WS_G + ((16 * w + n) * 32 + q) * 1152 + (ub + u) * 32 + g * 8;
    __builtin_amdgcn_raw_buffer_store_b64((u32x2){pk_bf(gc[0], gc[1]), pk_bf(gc[2], gc[3])}, rw2, o, 0, 16);
    if (hasS) __builtin_amdgcn_raw_buffer_store_b64((u32x2){pk_bf(gs[0], gs[1]), pk_bf(gs[2], gs[3])}, rw2, o + 16 * 1152, 0, 16);
  }
}

constexpr int K2_ST = 34816, K2_NS = 4, K2_LDS = K2_ST * K2_NS;
struct Frag2 { bf16x8 a[2], b[9]; };
__global__ __launch_bounds__(512, 2) void k2(const unsigned short* w2, char* ws) {
  extern __shared__ __attribute__((aligned(1024))) char smem[];
  int b = blockIdx.x, xcd = b & 7, idx = b >> 3;
  int p = (xcd >> 2) * 8 + (idx >> 2), fq = xcd & 3, nt = idx & 3;
  int w = __builtin_amdgcn_readfirstlane(threadIdx.x >> 6), lane = threadIdx.x & 63, g = lane >> 4, n = lane & 15;
  int mq = w & 3, kh = w >> 2;
  const u32x4 rh = mk_rsrc(ws, WS_END), rw = mk_rsrc(w2, 4608 * 1152);
  const unsigned voffH = WS_H + ((8 * p + w) * 144 + 36 * fq) * 1024 + lane * 16;
  auto boff = [&](int ins) { int ci = ins * 64 + lane, row = ci / 18, pos = ci - row * 18; return (unsigned)((1152 * fq + row) * 1152 + nt * 288 + pos * 16); };
  const unsigned vb0 = boff(w), vb1 = boff(w + 8), vb2 = boff(w + 16);
  auto issue = [&](int st, int slot) { unsigned lb = slot * K2_ST;
    dma16(lb + w * 2048, voffH, rh, st * 2048); dma16(lb + w * 2048 + 1024, voffH, rh, st * 2048 + 1024);
    dma16(lb + 16384 + w * 1024, vb0, rw, st * (64 * 1152)); dma16(lb + 16384 + 8192 + w * 1024, vb1, rw, st * (64 * 1152));
    if (w < 2) dma16(lb + 16384 + 16384 + w * 1024, vb2, rw, st * (64 * 1152)); };
  for (int s = 0; s < K2_NS; ++s) issue(s, s);
  const unsigned rdA = mq * 4096 + kh * 1024 + lane * 16;
  const unsigned rdB = 16384 + (32 * kh + 4 * g + (n >> 2)) * 288 + 8 * (n & 3);
  f32x4 acc[2][9];
#pragma unroll
  for (int u = 0; u < 9; ++u) acc[0][u] = acc[1][u] = (f32x4){0, 0, 0, 0};
  auto rd = [&](Frag2& F, int slot) { unsigned so = slot * K2_ST;
    F.a[0] = lds_b128(rdA + so); F.a[1] = lds_b128(rdA + so + 2048);
#pragma unroll
    for (int u = 0; u < 9; ++u) F.b[u] = lds_tr(rdB + so + u * 32, rdB + so + u * 32 + 16 * 288); };
  auto mm = [&](const Frag2& F) {
#pragma unroll
    for (int u = 0; u < 9; ++u) { acc[0][u] = mfma_bf(F.b[u], F.a[0], acc[0][u]); acc[1][u] = mfma_bf(F.b[u], F.a[1], acc[1][u]); } };
  Frag2 F0, F1;
  WAITV(4 * (K2_NS - 1)); BARRIER();
  rd(F0, 0);
  for (int i = 0; i < 18; i += 2) {
    if (i + K2_NS <= 18) WAITV(4 * (K2_NS - 2)); else WAITV(0);
    BARRIER(); SCHED();
    if (i + K2_NS < 18) issue(i + K2_NS, i & 3);
    rd(F1, (i + 1) & 3);
    mm(F0); SCHED();
    if (i + 1 + K2_NS <= 18) WAITV(4 * (K2_NS - 2)); else WAITV(0);
    BARRIER(); SCHED();
    if (i + 1 + K2_NS < 18) issue(i + 1 + K2_NS, (i + 1) & 3);
    if (i < 16) rd(F0, (i + 2) & 3);
    mm(F1); SCHED();
  }
  BARRIER();
#pragma unroll
  for (int u = 0; u < 9; ++u) *(LAS f32x4*)(unsigned)((w * 9 + u) * 1024 + lane * 16) = kh ? acc[0][u] : acc[1][u];
  BARRIER();
  __amdgpu_buffer_rsrc_t rws = __builtin_amdgcn_make_buffer_rsrc(ws, 0, WS_END, 0x00020000);
#pragma unroll
  for (int u = 0; u < 9; ++u) { f32x4 o = *(LAS f32x4*)(unsigned)(((w ^ 4) * 9 + u) * 1024 + lane * 16); o += kh ? acc[1][u] : acc[0][u];
    __builtin_amdgcn_raw_buffer_store_b64((u32x2){pk_bf(o[0], o[1]), pk_bf(o[2], o[3])}, rws, WS_P + ((fq * 128 + 8 * p + 2 * mq + kh) * 36 + 9 * nt + u) * 512 + lane * 8, 0, 16); }
}

__global__ __launch_bounds__(576) void k3(const unsigned short* x, const unsigned short* b2, const char* ws, unsigned short* out) {
  __shared__ __attribute__((aligned(1024))) char smem[36864];
  int b = blockIdx.x, j = b >> 2, nt = b & 3;
  int w = __builtin_amdgcn_readfirstlane(threadIdx.x >> 6), lane = threadIdx.x & 63, g = lane >> 4, n = lane & 15;
  unsigned lb = (unsigned)(size_t)smem;
  const u32x4 rws = mk_rsrc(ws, WS_END);
#pragma unroll
  for (int i = 0; i < 4; ++i) { int row = 8 * i + (lane >> 3);
    dma16(lb + w * 4096 + i * 1024, WS_G + (j * 32 + row) * 1152 + (((lane & 7) ^ ((row >> 1) & 7)) << 4), rws, w * 128); }
  int unit = 9 * nt + w;
  bf16x8 d[18];
#pragma unroll
  for (int kb = 0; kb < 18; ++kb) d[kb] = *(const bf16x8*)(TABP + ct::OFF_CI + unit * 18432 + kb * 1024 + lane * 16);
  u32x2 pp[2][4], xv[2];
#pragma unroll
  for (int rt = 0; rt < 2; ++rt) {
#pragma unroll
    for (int ks = 0; ks < 4; ++ks) pp[rt][ks] = *(const u32x2*)(ws + WS_P + ((ks * 128 + 2 * j + rt) * 36 + unit) * 512 + lane * 8);
    xv[rt] = *(const u32x2*)(x + (j + 64 * (16 * rt + n)) * 576 + unit * 16 + 4 * g); }
  u32x2 bb = *(const u32x2*)(b2 + unit * 16 + 4 * g);
  bf16x8 hi[2];
#pragma unroll
  for (int rt = 0; rt < 2; ++rt) hi[rt] = *(const bf16x8*)(TABP + ct::OFF_HS + 2048 + rt * 1024 + lane * 16);
  WAITV(0); BARRIER();
  f32x4 p0 = {0, 0, 0, 0}, p1 = {0, 0, 0, 0};
  const unsigned ra = lb + n * 128 + ((g ^ ((n >> 1) & 7)) << 4);
#pragma unroll
  for (int kb = 0; kb < 18; ++kb) { unsigned a = (ra ^ ((kb & 1) << 6)) + (kb >> 1) * 4096;
    p0 = mfma_bf(lds_b128(a), d[kb], p0); p1 = mfma_bf(lds_b128(a + 2048), d[kb], p1); }
  u32x4 pk = {pk_bf(p0[0], p0[1]), pk_bf(p0[2], p0[3]), pk_bf(p1[0], p1[1]), pk_bf(p1[2], p1[3])};
  float bias[4] = {bf_lo(bb[0]), bf_hi(bb[0]), bf_lo(bb[1]), bf_hi(bb[1])};
#pragma unroll
  for (int rt = 0; rt < 2; ++rt) {
    f32x4 y = mfma_bf(__builtin_bit_cast(bf16x8, pk), hi[rt], (f32x4){0, 0, 0, 0});
    float o[4];
#pragma unroll
    for (int r = 0; r < 4; ++r) { float s = 0;
#pragma unroll
      for (int ks = 0; ks < 4; ++ks) s += (r & 1) ? bf_hi(pp[rt][ks][r >> 1]) : bf_lo(pp[rt][ks][r >> 1]);
      float z = rbf(rbf(s) + bias[r]);
      float gt = rbf(z / (1.f + __expf(-z)));
      float xx = (r & 1) ? bf_hi(xv[rt][r >> 1]) : bf_lo(xv[rt][r >> 1]);
      o[r] = xx + rbf(rbf(y[r]) * gt); }
    *(u32x2*)(out + (j + 64 * (16 * rt + n)) * 576 + unit * 16 + 4 * g) = (u32x2){pk_bf(o[0], o[1]), pk_bf(o[2], o[3])};
  }
}

extern "C" void kernel_launch(void* const* d_in, const int* in_sizes, int n_in, void* d_out, int out_size, void* d_ws, size_t ws_size, hipStream_t stream) {
  const unsigned short* x = (const unsigned short*)d_in[0]; const unsigned short* wr = (const unsigned short*)d_in[1]; const unsigned short* wi = (const unsigned short*)d_in[2];
  const unsigned short* w1 = (const unsigned short*)d_in[4]; const unsigned short* b1 = (const unsigned short*)d_in[5];
  const unsigned short* w2 = (const unsigned short*)d_in[6]; const unsigned short* b2 = (const unsigned short*)d_in[7];
  char* ws = (char*)d_ws;
  (void)hipFuncSetAttribute((const void*)k1, hipFuncAttributeMaxDynamicSharedMemorySize, K1_LDS);
  (void)hipFuncSetAttribute((const void*)k2, hipFuncAttributeMaxDynamicSharedMemorySize, K2_LDS);
  k0<<<512 + 18, 256, 0, stream>>>(x, w1, ws);
  k1<<<256, 512, K1_LDS, stream>>>(x, w1, b1, ws);
  kmid<<<204, 256, 0, stream>>>(wr, wi, ws);
  k2<<<256, 512, K2_LDS, stream>>>(w2, ws);
  k3<<<256, 576, 0, stream>>>(x, b2, ws, (unsigned short*)d_out);
}
```

```cpp
#include <hip/hip_runtime.h>
#include <utility>

typedef __attribute__((ext_vector_type(8))) __bf16 bf16x8;
typedef __attribute__((ext_vector_type(4))) __bf16 bf16x4;
typedef __attribute__((ext_vector_type(2))) __bf16 bf16x2;
typedef __attribute__((ext_vector_type(8))) _Float16 f16x8;
typedef __attribute__((ext_vector_type(2))) _Float16 f16x2;
typedef __attribute__((ext_vector_type(2))) float f32x2;
typedef __attribute__((ext_vector_type(4))) float f32x4;
typedef __attribute__((ext_vector_type(4))) unsigned u32x4;
typedef __attribute__((ext_vector_type(2))) unsigned u32x2;
typedef __attribute__((ext_vector_type(3))) unsigned u32x3;
typedef __attribute__((ext_vector_type(4))) short s16x4;
typedef unsigned short u16;
#define LAS __attribute__((address_space(3)))
#define DI __device__ __forceinline__
namespace ct {
constexpr double PI = 3.14159265358979323846264338327950288;
constexpr double cos_r(double x) { double t = 1, s = 1; for (int i = 1; i <= 14; ++i) { t *= -x * x / ((2 * i - 1) * (2 * i)); s += t; } return s; }
constexpr double cosn(int i, int n) {
  i %= n; if (2 * i > n) i = n - i; if (4 * i == n) return 0.0;
  if (4 * i > n) return -cos_r(PI * (n - 2 * i) / n);
  return cos_r(2 * PI * i / n); }
constexpr unsigned short f2bf(double d) { float f = (float)d; unsigned u = __builtin_bit_cast(unsigned, f); u += 0x7fffu + ((u >> 16) & 1u); return (unsigned short)(u >> 16); }
constexpr unsigned short f2h(double d) { return __builtin_bit_cast(unsigned short, (_Float16)d); }
constexpr float bf2f(unsigned short b) { return __builtin_bit_cast(float, (unsigned)b << 16); }
struct Lut576 { unsigned short v[576]; };
constexpr Lut576 mk576() { Lut576 l{}; for (int i = 0; i < 576; ++i) l.v[i] = f2bf(cosn(i, 576)); return l; }
constexpr Lut576 L576 = mk576();
struct Lut2048 { unsigned short h[2048], b[2048], s[2048]; };
constexpr Lut2048 mk2048() { Lut2048 l{}; for (int i = 0; i < 2048; ++i) { double c = cosn(i, 2048); l.h[i] = f2h(c); l.b[i] = f2bf(c); l.s[i] = f2bf(c * (2.0 / (576.0 * 2048.0))); } return l; }
constexpr Lut2048 L2048 = mk2048();
constexpr unsigned short cval(int d, int kk) { return kk == 1 ? L576.v[(d & 1) * 288] : L576.v[((kk >> 1) * d + (kk & 1) * 144) % 576]; }
struct Unit { unsigned short v[18 * 512]; };
constexpr Unit mkunit(int u) { Unit r{}; for (int ks = 0; ks < 18; ++ks) for (int l = 0; l < 64; ++l) for (int i = 0; i < 8; ++i) {
  int a = 32 * ks + 8 * (l >> 4) + i, b = 16 * u + (l & 15); r.v[(ks * 64 + l) * 8 + i] = cval(a, b); } return r; }
template <int U> struct UH { static constexpr Unit v = mkunit(U); };
constexpr int pk(int g, int i) { return 16 * (i >> 2) + 4 * g + (i & 3); }
constexpr int ksof(int q, int m) { return q ? q + 32 * m : (m < 32 ? 32 * m : 32 * m - 1008); }
constexpr int midx(int q, int m, int j, int cs) { return (q == 0 && m == 0 && cs) ? (j & 1) * 1024 : (j * ksof(q, m) + cs * 1536) & 2047; }
struct MQ { unsigned short f[16 * 512]; unsigned short i[16 * 512]; };
constexpr MQ mkmq(int q) { MQ r{};
  for (int cs = 0; cs < 2; ++cs) for (int mt = 0; mt < 4; ++mt) for (int jk = 0; jk < 2; ++jk) for (int l = 0; l < 64; ++l) for (int i = 0; i < 8; ++i)
    r.f[(((mt * 2 + cs) * 2 + jk) * 64 + l) * 8 + i] = L2048.h[midx(q, 16 * mt + (l & 15), 32 * jk + pk(l >> 4, i), cs)];
  for (int jt = 0; jt < 4; ++jt) for (int mt = 0; mt < 4; ++mt) for (int l = 0; l < 64; ++l) for (int i = 0; i < 8; ++i)
    r.i[((jt * 4 + mt) * 64 + l) * 8 + i] = L2048.s[midx(q, 16 * mt + 4 * (l >> 4) + (i & 3), 16 * jt + (l & 15), i >> 2)];
  return r; }
template <int Q> struct MH { static constexpr MQ v = mkmq(Q); };
constexpr int hidx(int rho, int r) { return rho <= 16 ? (64 * rho * r) & 2047 : (64 * (rho - 16) * r + 1536) & 2047; }
struct HS { unsigned short f[2 * 512]; unsigned short i[2 * 512]; unsigned short cs[576 * 4]; };
constexpr HS mkhs() { HS r{};
  for (int t = 0; t < 2; ++t) for (int l = 0; l < 64; ++l) for (int i = 0; i < 8; ++i) {
    r.f[(t * 64 + l) * 8 + i] = L2048.h[hidx(16 * t + (l & 15), pk(l >> 4, i))];
    r.i[(t * 64 + l) * 8 + i] = L2048.b[hidx(pk(l >> 4, i), 16 * t + (l & 15))]; }
  const int ks[5] = {0, 128, 256, 384, 512};
  for (int a = 0; a < 5; ++a) { double s = 0; for (int d = 0; d < 576; ++d) s += bf2f(cval(d, ks[a])); r.cs[4 * ks[a]] = f2bf(s); r.cs[4 * ks[a] + 1] = f2bf(s - bf2f(f2bf(s))); }
  return r; }
struct Tab { Unit cf[36]; MQ mq[16]; HS hs; };
template <int... I, int... Q> constexpr Tab mktab(std::integer_sequence<int, I...>, std::integer_sequence<int, Q...>) {
  return Tab{{UH<I>::v...}, {MH<Q>::v...}, mkhs()}; }
constexpr unsigned OFF_CF = 0, OFF_MQ = 36 * 18432, OFF_HS = OFF_MQ + 16 * 32768, TAB_BYTES = OFF_HS + 4096 + 4608;
static_assert(sizeof(Tab) == TAB_BYTES, "tab");
}
#if defined(__HIP_DEVICE_COMPILE__)
__device__ __attribute__((aligned(1024))) const ct::Tab g_tab = ct::mktab(std::make_integer_sequence<int, 36>{}, std::make_integer_sequence<int, 16>{});
#define TABP ((const char*)&g_tab)
#else
#define TABP ((const char*)nullptr)
#endif
constexpr unsigned WS_EPOCH = 0;
constexpr unsigned WS_STAT = 4096;
constexpr unsigned WS_CS   = WS_STAT + 16384;
constexpr unsigned WS_F1   = WS_CS + 3 * 36864;
constexpr unsigned WS_F2   = WS_F1 + 32768;
constexpr unsigned WS_FP   = WS_F2 + 32768;
constexpr unsigned WS_E    = 524288;
constexpr unsigned WS_G    = WS_E + 2359296;
constexpr unsigned WS_H    = WS_G + 2359296;
constexpr unsigned WS_P    = WS_H + 18874368;
constexpr unsigned WS_PL   = WS_P + 8388608;
constexpr unsigned WS_WF   = WS_PL + 4194304;
constexpr unsigned WS_END  = WS_WF + 16 * 19 * 8192;
constexpr unsigned OOB = 0xf0000000u;

#define STAMP_DECL
#define STAMP(i)
#define DSTAMP(i)
#define STAMP_FLUSH()

typedef __amdgpu_buffer_rsrc_t rsrc_t;
DI u32x4 mk_rsrc(const void* p, unsigned bytes) { unsigned long long a = (unsigned long long)p; return (u32x4){(unsigned)a, (unsigned)(a >> 32) & 0xffffu, bytes, 0x00020000u}; }
DI rsrc_t mk_rs(const void* p, unsigned bytes) { return __builtin_amdgcn_make_buffer_rsrc((void*)p, 0, bytes, 0x00020000); }
#define DMA_(op, lds, voff, rsrc, soff) asm volatile("s_mov_b32 m0, %0\n\ts_nop 0\n\t" op :: "s"(lds), "v"(voff), "s"(rsrc), "s"(soff) : "memory")
DI void dma16(unsigned lds, unsigned voff, u32x4 rsrc, unsigned soff) { DMA_("buffer_load_dwordx4 %1, %2, %3 offen lds", lds, voff, rsrc, soff); }
DI void dma4(unsigned lds, unsigned voff, u32x4 rsrc, unsigned soff) { DMA_("buffer_load_dword %1, %2, %3 offen lds", lds, voff, rsrc, soff); }
DI void dma16c(unsigned lds, unsigned voff, u32x4 rsrc, unsigned soff) { DMA_("buffer_load_dwordx4 %1, %2, %3 offen sc1 lds", lds, voff, rsrc, soff); }
template <unsigned LI, unsigned SI, bool C> DI void dmai(unsigned lb, unsigned voff, u32x4 rsrc, unsigned sb) { unsigned t;
  if (C) asm volatile("s_add_u32 m0, %1, %5\n\ts_add_u32 %0, %4, %6\n\tbuffer_load_dwordx4 %2, %3, %0 offen sc1 lds" : "=&s"(t) : "s"(lb), "v"(voff), "s"(rsrc), "s"(sb), "n"(LI), "n"(SI) : "memory", "scc");
  else asm volatile("s_add_u32 m0, %1, %5\n\ts_add_u32 %0, %4, %6\n\tbuffer_load_dwordx4 %2, %3, %0 offen lds" : "=&s"(t) : "s"(lb), "v"(voff), "s"(rsrc), "s"(sb), "n"(LI), "n"(SI) : "memory", "scc"); }
#define LD128(r, vo, so) __builtin_amdgcn_raw_buffer_load_b128(r, vo, so, 0)
#define LD96(r, vo, so) __builtin_amdgcn_raw_buffer_load_b96(r, vo, so, 0)
#define LD64(r, vo, so) __builtin_amdgcn_raw_buffer_load_b64(r, vo, so, 0)
#define LD32(r, vo, so) __builtin_amdgcn_raw_buffer_load_b32(r, vo, so, 0)
#define LD128C(r, vo, so) __builtin_amdgcn_raw_buffer_load_b128(r, vo, so, 16)
#define LD64C(r, vo, so) __builtin_amdgcn_raw_buffer_load_b64(r, vo, so, 16)
DI int lane_id() { int l; asm volatile("v_mbcnt_lo_u32_b32 %0, -1, 0\n\tv_mbcnt_hi_u32_b32 %0, -1, %0" : "=v"(l)); return l; }
#define PIN(x) asm volatile("" : "+v"(x))
#define WAITV(n) asm volatile("s_waitcnt vmcnt(%0)" :: "n"(n) : "memory")
struct Tr {};
#define TMARK()
#define TFLUSH(slot)
#define BARRIER() do { __builtin_amdgcn_s_waitcnt(0xC07F); TMARK(); asm volatile("s_barrier" ::: "memory"); } while (0)
#define SCHED() __builtin_amdgcn_sched_barrier(0)
DI bf16x8 lds_b128(unsigned a) { return *(LAS bf16x8*)(size_t)a; }
DI f32x4 lds_f4(unsigned a) { return *(LAS f32x4*)(size_t)a; }
DI bf16x8 lds_tr(unsigned a0, unsigned a1) {
  bf16x4 lo = __builtin_amdgcn_ds_read_tr16_b64_v4bf16((LAS bf16x4*)(size_t)a0), hi = __builtin_amdgcn_ds_read_tr16_b64_v4bf16((LAS bf16x4*)(size_t)a1);
  return __builtin_shufflevector(lo, hi, 0, 1, 2, 3, 4, 5, 6, 7); }
DI u32x4 lds_tru(unsigned a0, unsigned a1) { return __builtin_bit_cast(u32x4, lds_tr(a0, a1)); }
DI f32x4 mfma_bf(bf16x8 a, bf16x8 b, f32x4 c) { return __builtin_amdgcn_mfma_f32_16x16x32_bf16(a, b, c, 0, 0, 0); }
DI f32x4 mfma_bfu(u32x4 a, u32x4 b, f32x4 c) { return mfma_bf(__builtin_bit_cast(bf16x8, a), __builtin_bit_cast(bf16x8, b), c); }
DI f32x4 mfma_hu(u32x4 a, u32x4 b, f32x4 c) { return __builtin_amdgcn_mfma_f32_16x16x32_f16(__builtin_bit_cast(f16x8, a), __builtin_bit_cast(f16x8, b), c, 0, 0, 0); }
DI f32x4 mfma16(u32x2 a, u32x2 b, f32x4 c) { return __builtin_amdgcn_mfma_f32_16x16x16bf16_1k(__builtin_bit_cast(s16x4, a), __builtin_bit_cast(s16x4, b), c, 0, 0, 0); }
DI u32x2 lds_u2(unsigned a) { return *(LAS u32x2*)(size_t)a; }
DI unsigned pk_bf(float a, float b) { return __builtin_bit_cast(unsigned, __builtin_convertvector((f32x2){a, b}, bf16x2)); }
DI unsigned pk_h(float a, float b) { return __builtin_bit_cast(unsigned, __builtin_convertvector((f32x2){a, b}, f16x2)); }
DI float bf_lo(unsigned u) { return __uint_as_float(u << 16); }
DI float bf_hi(unsigned u) { return __uint_as_float(u & 0xffff0000u); }
DI float rbf(float x) { return bf_lo(pk_bf(x, 0.f)); }
DI u32x2 pk4(f32x4 v) { return (u32x2){pk_bf(v[0], v[1]), pk_bf(v[2], v[3])}; }
DI u32x4 pk8(f32x4 a, f32x4 b) { return (u32x4){pk_bf(a[0], a[1]), pk_bf(a[2], a[3]), pk_bf(b[0], b[1]), pk_bf(b[2], b[3])}; }
DI u32x2 relu2(f32x4 v) { f32x4 t;
#pragma unroll
  for (int r = 0; r < 4; ++r) t[r] = __int_as_float(max(__float_as_int(v[r]), 0));
  return pk4(t * t); }
template <int C> DI float dpp(float x) { return __builtin_bit_cast(float, __builtin_amdgcn_update_dpp(0, __builtin_bit_cast(int, x), C, 0xf, 0xf, true)); }
DI float rows_sum(float v) {
  int i = __builtin_bit_cast(int, v);
  return (__builtin_bit_cast(float, __builtin_amdgcn_readlane(i, 15)) + __builtin_bit_cast(float, __builtin_amdgcn_readlane(i, 31))) +
         (__builtin_bit_cast(float, __builtin_amdgcn_readlane(i, 47)) + __builtin_bit_cast(float, __builtin_amdgcn_readlane(i, 63))); }
DI void st128c(u32x4 v, u32x4 r, unsigned vo, unsigned so) { asm volatile("buffer_store_dwordx4 %0, %1, %2, %3 offen sc1\n\ts_nop 1" :: "v"(v), "v"(vo), "s"(r), "s"(so) : "memory"); }
#define ST128(v, r, vo, so) st128c(v, rwd, vo, so)
DI void st64c(u32x2 v, u32x4 r, unsigned vo, unsigned so) { asm volatile("buffer_store_dwordx2 %0, %1, %2, %3 offen sc1" :: "v"(v), "v"(vo), "s"(r), "s"(so) : "memory"); }
#define ST64(v, r, vo, so) st64c(v, rwd, vo, so)
DI float fadd(float a, float b) { float r; asm("v_add_f32 %0, %1, %2" : "=v"(r) : "v"(a), "v"(b)); return r; }
DI float fsub(float a, float b) { float r; asm("v_sub_f32 %0, %1, %2" : "=v"(r) : "v"(a), "v"(b)); return r; }
DI float fmac(float c, float a, float b) { asm("v_fmac_f32 %0, %1, %2" : "+v"(c) : "v"(a), "v"(b)); return c; }
DI float rev576(float x) { return fmaf(-576.f, __builtin_floorf(x * (1.f / 576.f)), x) * (1.f / 576.f); }
constexpr unsigned SPIN_CAP = 1u << 14;
template <int N> struct Flags {
  rsrc_t r; unsigned vo, S; u32x2 tag, f[N];
  DI void look() { asm volatile("" ::: "memory");
#pragma unroll
    for (int i = 0; i < N; ++i) f[i] = LD64C(r, vo, i * S);
    asm volatile("" ::: "memory"); }
  DI bool ok() { bool o = true;
#pragma unroll
    for (int i = 0; i < N; ++i) o = o && f[i][0] == tag[0] && f[i][1] == tag[1];
    return __all(o || vo >= OOB); }
  template <int NAP = 4> DI void await() {
#pragma nounroll
    for (unsigned it = 0; it < SPIN_CAP && !ok(); ++it) { __builtin_amdgcn_s_sleep(NAP); look(); }
    asm volatile("" ::: "memory"); }
};

template <typename F, int... I> DI void static_for_(F f, std::integer_sequence<int, I...>) { (f(std::integral_constant<int, I>{}), ...); }
template <int N, typename F> DI void static_for(F f) { static_for_(f, std::make_integer_sequence<int, N>{}); }
#define HPOL 0
#define QN 4
#define KMV 8
constexpr int KM = KMV;
template <bool MID> struct PlanL {
  int n[40] = {}, it[40][12] = {};
  static constexpr int len(int c) { return c < 200 ? 10 : c < 350 ? 4 : c < 400 ? 2 : c < 450 ? 4 : 8; }
  constexpr void add(int T, int c) { it[T + 1][n[T + 1]++] = c; }
  constexpr PlanL() {
    int st[40] = {}, ns = 0;
    for (int s = 0; s < 18; ++s) { if (s >= 6) st[ns++] = 200 + s; else if (!(s & 1)) { st[ns++] = 200 + s; st[ns++] = 201 + s; } st[ns++] = 100 + s; }
    int h = 0;
    for (; h < 4; ++h) add(-1, st[h]);
    for (; h < 6; ++h) add(0, st[h]);
    for (int i = 0; i < 18; ++i) for (int q = 0; h < ns; ++h) { const int c = st[h];
      if ((c < 200 ? c - 100 - 4 : 2 * ((c - 200) / 2) - 4) > i || q + len(c) > (i < QN ? 24 : 14)) break;
      add(i + 1, c); q += len(c); }
    add(15, 305); add(15, 306); add(15, 350);
    for (int s = 1; s < 6; ++s) add(16, 350 + s);
    add(18, 300); add(18, 301);
    add(19, 356); add(19, 302); add(19, 303); add(19, 304);
    add(21, 307); add(21, 308); add(21, 357); add(21, 358); add(21, 309); add(21, 310);
    add(22, 311); add(22, 312); if (HPOL) { add(23, 409); add(23, 410); } else for (int s = 9; s < 13; ++s) add(22, 400 + s);
    for (int k = 3, s = 13, sh = HPOL ? 11 : 13; k < 15; ++k) {
      if (MID && k == KM - 3) add(20 + k, 450);
      const int lag = !MID ? 0 : k == KM - 3 ? 1 : (k == KM - 2 || k == KM - 1) ? 2 : 0;
      if (HPOL && (k == 4 || k == KM)) add(20 + k, 400 + sh++);
      for (; s < 2 * k + 9 - lag && s < 36; ++s) { add(20 + k, 400 + sh++); add(20 + k, 300 + s); } } }
  constexpr int pos(int c) const { int t = 0; for (int r = 0; r < 40; ++r) for (int k = 0; k < n[r]; ++k) { t += len(it[r][k]); if (it[r][k] == c) return t; } return 0; }
  constexpr int before(int T) const { int t = 0; for (int r = 0; r <= T; ++r) for (int k = 0; k < n[r]; ++k) t += len(it[r][k]); return t; }
  constexpr int upto(int T, int c) const { int r = before(T) - pos(c); return r < 0 ? -1000 : r > 63 ? 63 : r; }
  constexpr int wait(int T) const {
    int need[5] = {}, m = 0;
    if (T <= 17) { need[m++] = 100 + T; need[m++] = 201 + 2 * (T / 2); }
    if (T == 20) need[m++] = 304;
    if (T > 20) { const int k = T - 20; for (int s = 2 * k + 1; s < 2 * k + 3 && s < 36; ++s) { need[m++] = 300 + s; need[m++] = 400 + s; }
      if (k == 3) need[m++] = 358;
      if (MID && k == KM) need[m++] = 450; }
    int r = 63; for (int i = 0; i < m; ++i) { int z = upto(T, need[i]); r = z < r ? z : r; }
    return r; }
};
#define FLAGI 3
#define YP 2
#define LNAP 6
#define GPOL 0
#define LPOL 0
constexpr unsigned LA = 0, LB = 49152, LBS = 21504, LMISC = 159744, LJUNK = 156672;
constexpr unsigned LH = 0, LW = 73728, LWL = 131072, LE = 143360;
constexpr unsigned LG = 0, LDS_BYTES = 163840;
DI unsigned xslot(int i) { return 36864 + i * 8192 + (i >= 3 ? 12288 : 0) + (i >= 6 ? 8192 : 0); }
struct Args { const u16 *x, *wr, *wi, *w1, *b1, *w2, *b2; u16* out; char* ws; };
struct Pos { int b, xcd, idx, p, fq, nt, c, q, ub4, j; };
DI Pos where() { Pos P; P.b = blockIdx.x; P.xcd = P.b & 7; P.idx = P.b >> 3; P.p = (P.xcd >> 2) * 8 + (P.idx >> 2); P.fq = P.xcd & 3; P.nt = P.idx & 3; P.c = 4 * P.fq + P.nt;
  const int mr = (P.idx >> 2) * 3 + P.nt - 1; P.q = 2 * P.xcd + (mr >= 9); P.ub4 = (mr >= 9 ? mr - 9 : mr) * 4; P.j = 4 * P.p + P.fq; return P; }
DI u32x2 get_tag(const char* ws) {
  unsigned long long z = *(const __attribute__((address_space(4))) unsigned long long*)(ws + WS_EPOCH) + 0x9E3779B97F4A7C15ull;
  z = (z ^ (z >> 31)) * 0xBF58476D1CE4E5B9ull; z ^= z >> 29; u32x2 t = {(unsigned)z, (unsigned)(z >> 32)}; asm volatile("" : "+s"(t)); return t; }
__device__ __attribute__((noinline, used)) void fused_start_marker() {}
__device__ void code_end_marker();
DI void code_pf(int lane, unsigned junk) {
  const unsigned long long pc = (unsigned long long)(const void*)&fused_start_marker;
  const long long len = (long long)(unsigned long long)(const void*)&code_end_marker - (long long)pc;
  dma4(junk, lane < 32 ? lane * 128 : OOB, mk_rsrc((const void*)pc, (len > 0 && len < 131072) ? (unsigned)len : 0u), (blockIdx.x >> 3) * 4096); }
#define BAR() do { TMARK(); asm volatile("s_barrier" ::: "memory"); } while (0)

template <typename F> DI void gen_ops(u32x4 (&dd)[18], int d0, int lane, F hook) {
  const int g = lane >> 4; const float df = (float)(d0 + (lane & 15)), k0 = (float)(4 * g) * df;
  float za[8], zb[8];
#pragma unroll
  for (int t = 0; t < 4; ++t) { float r0 = rev576(fmaf((float)t, df, k0)), r1 = rev576(fmaf((float)(t + 16), df, k0));
    za[2 * t] = __builtin_amdgcn_cosf(r0); za[2 * t + 1] = __builtin_amdgcn_sinf(-r0); zb[2 * t] = __builtin_amdgcn_cosf(r1); zb[2 * t + 1] = __builtin_amdgcn_sinf(-r1); }
  const float c2 = 2.f * __builtin_amdgcn_cosf(rev576(16.f * df)); za[1] = g ? za[1] : 0.f;
#pragma unroll
  for (int i = 0; i < 9; ++i) {
    dd[2 * i] = (u32x4){pk_bf(za[0], za[1]), pk_bf(za[2], za[3]), pk_bf(za[4], za[5]), pk_bf(za[6], za[7])};
    dd[2 * i + 1] = (u32x4){pk_bf(zb[0], zb[1]), pk_bf(zb[2], zb[3]), pk_bf(zb[4], zb[5]), pk_bf(zb[6], zb[7])};
    if (i < 8) {
#pragma unroll
      for (int t = 0; t < 8; ++t) { za[t] = fmaf(c2, zb[t], -za[t]); zb[t] = fmaf(c2, za[t], -zb[t]); } }
    PIN(dd[2 * i]); PIN(dd[2 * i + 1]);
    hook(i); }
  dd[0][0] |= g ? 0u : ((lane & 1) ? 0xbf800000u : 0x3f800000u);
}
template <typename F> DI u32x4 idft(const u32x4 (&dd)[18], int lane, F hook) {
  f32x4 p0 = {0, 0, 0, 0}, p1 = p0, p2 = p0, p3 = p0;
  unsigned ra = LG + (lane & 15) * 128 + (((lane >> 4) ^ ((lane >> 1) & 7)) << 4), rb = ra ^ 64; PIN(ra); PIN(rb);
#pragma unroll
  for (int i = 0; i < 9; ++i) {
    p0 = mfma_bf(lds_b128(ra + i * 4096), __builtin_bit_cast(bf16x8, dd[2 * i]), p0); p1 = mfma_bf(lds_b128(ra + i * 4096 + 2048), __builtin_bit_cast(bf16x8, dd[2 * i]), p1);
    p2 = mfma_bf(lds_b128(rb + i * 4096), __builtin_bit_cast(bf16x8, dd[2 * i + 1]), p2); p3 = mfma_bf(lds_b128(rb + i * 4096 + 2048), __builtin_bit_cast(bf16x8, dd[2 * i + 1]), p3);
    if (i % 3 == 2) { asm volatile("" : "+v"(p0), "+v"(p1), "+v"(p2), "+v"(p3)); SCHED(); hook(i / 3); } }
  return pk8(p0 + p2, p1 + p3); }
DI f32x4 un4(u32x2 v) { return (f32x4){bf_lo(v[0]), bf_hi(v[0]), bf_lo(v[1]), bf_hi(v[1])}; }
DI u32x2 gate(f32x4 y, f32x4 s, float rs, u32x2 xx, u32x2 bs) {
  const f32x4 x = un4(xx), b = un4(bs); f32x4 o;
#pragma unroll
  for (int r = 0; r < 4; ++r) { const float z = fmaf(s[r], rs * rs, b[r]); o[r] = fmaf(y[r] * z, __builtin_amdgcn_rcpf(1.f + __expf(-z)), x[r]); }
  return pk4(o); }

DI void loader(const Args& A, const int l, const bool mid, Tr& tr) {
  const int lane = lane_id();
  const Pos P = where();
  const int g = lane >> 4, n = lane & 15;
  const u32x4 rx = mk_rsrc(A.x, 2048 * 1152), rw1 = mk_rsrc(A.w1, 576 * 9216), rt = mk_rsrc(TABP, ct::TAB_BYTES), rw2 = mk_rsrc(A.w2, 4608 * 1152), rwd = mk_rsrc(A.ws, WS_END);
  const unsigned l16 = lane * 16, sw8 = ((lane & 7) ^ ((4 * l + g) & 7)) << 4;
  const unsigned vA = (4 * P.p + 64 * (8 * l + (lane >> 3))) * 1152 + sw8;
  unsigned vB[9];
#pragma unroll
  for (int u = 0; u < 9; ++u) { int ci = u * 64 + lane, row = (ci * 1821) >> 16, pos = ci - row * 36; vB[u] = (row + 16 * l) * 9216 + P.c * 576 + ((pos ^ (((row >> 3) & 1) << 1)) << 4); }
  const bool ex0 = P.nt == 0 && l == 0;
  const unsigned wrow = (1152 * P.fq + 16 * l) * 1152;
  const unsigned vW[2] = {wrow + g * 1152 + P.nt * 256 + ((n ^ (g << 1)) << 4), wrow + (4 + g) * 1152 + P.nt * 256 + ((n ^ ((4 + g) << 1)) << 4)};
  const unsigned vL = wrow + (lane >> 3) * 1152 + 1024 + (((lane & 7) ^ (g << 1)) << 4);
  unsigned wb[4], hb[3], zero = 0, l1 = l * 1024, l2 = l * 2048, l4 = l * 4096, l9 = l * 9216;
#pragma unroll
  for (int i = 0; i < 4; ++i) { const int t9 = 9 * ((P.nt + 1 + i) & 3); wb[i] = t9 * (32 * 1152); if (i < 3) hb[i] = WS_H + ((8 * P.p + 4 * l) * 144 + 36 * P.fq + t9) * 1024; }
  const unsigned cb = ct::OFF_CF + (2 * P.c + l) * 18432, eb = ct::OFF_CF + (32 + P.fq) * 18432;
  asm volatile("" : "+s"(zero));
#define ROT 0
  const int rot = ROT ? ((P.idx >> 2) + 2 * P.nt) % 9 : 0;
  auto ka = [&](int a) { int t = a + rot; t = t >= 9 ? t - 9 : t; asm volatile("" : "+s"(t)); return (unsigned)t; };
  auto group = [&](auto CC) { constexpr int c = CC;
    if constexpr (c < 200) { constexpr unsigned s = c - 100, ld = LB + (s % 5) * LBS;
      const unsigned t = ka(s >> 1), tw = t * (64 * 9216);
      if (ex0) dmai<ld + 20480, (s & 1) * 1024, false>(zero, l16, rt, eb + t * 2048);
      static_for<9>([&](auto U) { dmai<ld + U * 1024, (s & 1) * (32 * 9216), false>(l9, vB[U], rw1, tw); });
      dmai<ld + 18432, (s & 1) * 1024, false>(l1, l16, rt, cb + t * 2048);
    } else if constexpr (c < 300) { constexpr unsigned a = (c - 200) >> 1, h = c & 1; const unsigned tx = ka(a) * 128;
      static_for<4>([&](auto I) { constexpr unsigned i = 4 * h + I; dmai<LA + (a % 3) * 16384 + i * 2048, (i >> 1) * 1152 + (i & 1) * (16 * 64 * 1152), false>(l1, vA, rx, tx); });
    } else if constexpr (c < 350) { constexpr unsigned s = c - 300, kb = s < 9 ? s : (s - 9) / 3;
      static_for<4>([&](auto I) { dmai<LW + (s & 7) * 8192 + I * 1024, kb * (32 * 1152) + (I >> 1) * (8 * 1152), false>(l4, vW[I & 1], rw2, wb[s < 9 ? 3 : (s - 9) % 3]); });
    } else if constexpr (c < 400) { constexpr unsigned s = c - 350;
      static_for<2>([&](auto I) { dmai<LWL + (s < 7 ? s : s - 5) * 4096 + I * 1024, s * (32 * 1152) + I * (8 * 1152), false>(l2, vL, rw2, wb[3]); });
    } else if constexpr (c < 450) { constexpr unsigned s = c - 400;
      static_for<4>([&](auto I) { dmai<LH + (s % 9) * 8192 + I * 1024, (s - 9) / 3 * 1024 + I * (144 * 1024), true>(l4, l16, rwd, hb[(s - 9) % 3]); });
    } else { const unsigned so = WS_E + (P.q * 36 + P.ub4) * 4096 + l * 8192;
      static_for<8>([&](auto I) { dmai<LE + I * 1024, I * 1024, true>(2 * l4, l16, rwd, so); });
    } };
  Flags<1> fh; fh.r = mk_rs(A.ws, WS_END); fh.S = 0; fh.vo = (lane < 4 && lane != P.nt) ? WS_F1 + ((4 * P.fq + lane) * 16 + P.p) * 128 : OOB;
#define LPRIO 3
  __builtin_amdgcn_s_setprio(LPRIO);
  bool early = false;
  auto row = [&](auto R, auto M) {
    constexpr int r = R; constexpr PlanL<M> PL{};
    auto first_h = [&]() { static_for<4>([&](auto I) { group(std::integral_constant<int, 409 + I>{}); }); };
    if (r == 24) { if (HPOL) fh.await(); else if (!early) { fh.await(); first_h(); } }
    static_for<PL.n[r]>([&](auto TT) { constexpr int c = PlanL<M>{}.it[r][TT];
      if (!HPOL && c == 311) fh.look();
      if (HPOL || c < 409 || c > 412) group(std::integral_constant<int, c>{}); });
    if (r == 23) { if (HPOL) { __builtin_amdgcn_s_sleep(LNAP); fh.look(); }
      else { early = fh.ok(); if (!early) { __builtin_amdgcn_s_sleep(5); fh.look(); early = fh.ok(); } if (early) first_h(); } }
    if (r == 1) fh.tag = get_tag(A.ws);
    if (HPOL || r != 23) WAITV(PL.wait(r));
    BAR(); };
  constexpr int R0 = 18 + KM, R1 = 22 + KM;
  static_for<R0>([&](auto R) { row(R, std::false_type{}); });
  if (mid) static_for<R1 - R0>([&](auto R) { row(std::integral_constant<int, R0 + R>{}, std::true_type{}); });
  else static_for<R1 - R0>([&](auto R) { row(std::integral_constant<int, R0 + R>{}, std::false_type{}); });
  static_for<36 - R1>([&](auto R) { row(std::integral_constant<int, R1 + R>{}, std::false_type{}); });
  Flags<1> fg; fg.r = fh.r; fg.tag = fh.tag; fg.S = 0; fg.vo = lane < 16 ? WS_F2 + 6144 + lane * 128 : OOB;
  WAITV(0); BAR();
  fg.look(); BAR();
  bool done = false;
  const unsigned vG = WS_G + ((8 * l + (lane >> 3)) * 64 + P.j) * 1152 + sw8;
#pragma nounroll
  for (int T = 38; T <= 39; ++T) {
    if (!done) {
      if (T == 39) fg.template await<24>();
      done = T == 39 || fg.ok();
      if (done) {
        static_for<18>([&](auto I) { dmai<LG + I * 2048, (I >> 1) * 128 + (I & 1) * (16 * 64 * 1152), true>(l1, vG, rwd, zero); });
        if (P.b == 0 && l == 0) ST64(fh.tag, rws, lane ? OOB : 0, WS_EPOCH);
      } else fg.look(); }
    if (T == 39) WAITV(0);
    BAR(); }
}

template <int KX> DI void worker(const Args& A, const bool mid, Tr& tr) {
  const int lane = lane_id();
  const Pos P = where();
  const u32x4 rwd = mk_rsrc(A.ws, WS_END);
  const rsrc_t rws = mk_rs(A.ws, WS_END);
  const u32x2 tag = get_tag(A.ws);
  const int r8 = lane >> 3, ch = lane & 7;
  int T = 0;
  auto upto = [&](int t) { for (; T < t; ++T) BARRIER(); };
  __builtin_amdgcn_s_setprio(2);
  const int k0 = 8 * P.b + 4 * KX, lr = (lane * 57) >> 9, lp = lane - 9 * lr;
  u32x3 fr[5], fi[5];
  auto fetchw = [&](int i) { const rsrc_t rr = mk_rs(A.wr, 2048 * 578), ri = mk_rs(A.wi, 2048 * 578);
    if (i >= 0 && i < 4) { fr[i & 3] = LD96(rr, lane * 8, ((k0 + i) * 578) & ~3); fi[i & 3] = LD96(ri, lane * 8, ((k0 + i) * 578) & ~3); }
    else if (i == 4) { const unsigned vt = lane < 36 ? ((lr * 578) & ~3) + (lp < 8 ? 512 + lp * 8 : 572) : OOB; fr[4] = LD96(rr, vt, k0 * 578); fi[4] = LD96(ri, vt, k0 * 578); } };
  auto settle = [&]() {
#pragma unroll
    for (int i = 0; i < 5; ++i) { PIN(fr[i]); PIN(fi[i]); } };
  upto(3);
  if (KX == 0) {
    const int cj = P.nt ? 3 * P.p + P.nt - 1 : 48 + P.p, cline = cj / 3, cpart = cj - 3 * cline;
    const rsrc_t r1 = mk_rs(A.w1, cj < 54 ? 576 * 9216 : 0);
    const unsigned vo = r8 * 9216 + ch * 16, so = cpart * (192 * 9216) + P.fq * 2304 + cline * 128;
    float s[8] = {0, 0, 0, 0, 0, 0, 0, 0};
    u32x4 v[6][4];
#pragma unroll
    for (int t = 0; t < 10; ++t) {
      if (t >= 4) {
#pragma unroll
        for (int i = 0; i < 4; ++i) { PIN(v[t >= 4 ? t - 4 : 0][i]);
#pragma unroll
          for (int e = 0; e < 4; ++e) { const unsigned d = v[t >= 4 ? t - 4 : 0][i][e]; s[2 * e] = fadd(s[2 * e], bf_lo(d)); s[2 * e + 1] = fadd(s[2 * e + 1], bf_hi(d)); } }
#pragma unroll
        for (int e = 0; e < 8; ++e) PIN(s[e]); }
      if (t < 6) {
#pragma unroll
        for (int i = 0; i < 4; ++i) v[t < 6 ? t : 0][i] = LD128(r1, vo, so + (t * 4 + i) * (8 * 9216));
        fetchw(t - 1); }
      upto(T + 1); }
    float o = 0;
#pragma unroll
    for (int e = 0; e < 8; ++e) { float t = s[e]; t += dpp<0x128>(t);
      auto a = __builtin_amdgcn_permlane16_swap(__float_as_uint(t), __float_as_uint(t), false, false); t = fadd(__uint_as_float(a[0]), __uint_as_float(a[1]));
      auto c = __builtin_amdgcn_permlane32_swap(__float_as_uint(t), __float_as_uint(t), false, false); t = fadd(__uint_as_float(c[0]), __uint_as_float(c[1]));
      o = r8 == e ? t : o; }
    settle();
    ST64(((u32x2){__float_as_uint(o), tag[1]}), rws, (ch * 8 + r8) * 8, cj < 54 ? WS_CS + (cpart * 4608 + P.fq * 1152 + cline * 64) * 8 : OOB);
  } else {
    ST64(tag, rws, lane < 2 ? lane * 2048 : OOB, P.c ? OOB : WS_F2 + P.p * 128);
    const int trow = 128 * P.p + 8 * P.c + r8;
    const rsrc_t rxb = mk_rs(A.x, 2048 * 1152);
    const unsigned vo = ((trow >> 5) + 64 * (trow & 31)) * 1152 + ch * 16;
    u32x4 v[9];
    float s1[2] = {0, 0}, s2[2] = {0, 0}, piv = 0;
#pragma unroll
    for (int t = 0; t < 10; ++t) {
      if (t < 3) {
#pragma unroll
        for (int i = 0; i < 3; ++i) v[(3 * t + i) % 9] = LD128(rxb, vo, (3 * t + i) * 128); }
      if (t == 4) { PIN(v[0]); piv = __uint_as_float((unsigned)__builtin_amdgcn_ds_bpermute((lane & 56) * 4, (int)(v[0][0] << 16))); PIN(piv); }
      if (t >= 5) {
#pragma unroll
        for (int i = 2 * (t - 5); i < 2 * (t - 5) + 2 && i < 9; ++i) { PIN(v[i % 9]);
#pragma unroll
          for (int e = 0; e < 4; ++e) { const float d0 = fsub(bf_lo(v[i % 9][e]), piv), d1 = fsub(bf_hi(v[i % 9][e]), piv);
            s1[0] = fadd(s1[0], d0); s1[1] = fadd(s1[1], d1); s2[0] = fmac(s2[0], d0, d0); s2[1] = fmac(s2[1], d1, d1); } }
        PIN(s1[0]); PIN(s1[1]); PIN(s2[0]); PIN(s2[1]); }
      fetchw(t - 2);
      if (t < 9) upto(T + 1); }
    auto red8 = [&](float t) { t += dpp<0xB1>(t); t += dpp<0x4E>(t); return t + dpp<0x141>(t); };
    const float m1 = red8(s1[0] + s1[1]) * (1.f / 576.f), m2 = red8(s2[0] + s2[1]) * (1.f / 576.f);
    const float var = rbf(fmaxf(fmaf(-m1, m1, m2), 0.f));
    settle(); WAITV(0);
    ST64(((u32x2){pk_bf(-(piv + m1), __builtin_amdgcn_sqrtf(rbf(var + 1e-5f))), tag[1]}), rws, ch ? OOB : r8 * 8, WS_STAT + (128 * P.p + 8 * P.c) * 8);
  }
  upto(14);
  {
    settle();
    auto mk = [&](u32x3 x, u32x3 y, unsigned sh) { return (u32x4){__builtin_amdgcn_alignbit(x[1], x[0], sh), __builtin_amdgcn_alignbit(x[2], x[1], sh), __builtin_amdgcn_alignbit(y[1], y[0], sh), __builtin_amdgcn_alignbit(y[2], y[1], sh)}; };
    unsigned dt = OOB;
#pragma unroll
    for (int i = 0; i < 4; ++i) {
      const int ks = k0 + i, r5 = ks & 31, hi = ks >> 5, qq = r5 > 16 ? 32 - r5 : r5 & 15;
      const bool neg = qq ? r5 > 16 : hi >= 32;
      const int m = qq ? (neg ? 63 - hi : hi) : (r5 ? (neg ? 95 - hi : 32 + hi) : (neg ? (64 - hi) & 31 : hi));
      const unsigned so = WS_WF + qq * (19 * 8192) + (neg ? 4096 : 0) + m * 64;
      ST128(mk(fr[i], fi[i], (i & 1) * 16), rws, (lane >> 2) * 8192 + (lane & 3) * 16, so);
      dt = lr == i ? so : dt; }
    ST128(mk(fr[4], fi[4], (lr & 1) * 16), rws, dt + (lp >> 2) * 8192 + (lp & 3) * 16, 16 * 8192);
  }
  upto(16);
  {
    const rsrc_t rb1 = mk_rs(A.b1, 9216);
    constexpr int NR = KX ? 1 : 2;
    const unsigned pr0 = KX ? (lane < 16 ? 128 + lane : OOB >> 4) : lane;
    u32x4 cs[NR][3], st; unsigned bb[NR];
    auto fetch = [&]() {
#pragma unroll
      for (int i = 0; i < NR; ++i)
#pragma unroll
        for (int t = 0; t < 3; ++t) cs[i][t] = LD128C(rws, pr0 * 16, WS_CS + (t * 4608 + 288 * P.c + 128 * i) * 8);
      if (KX) st = LD128C(rws, lane * 16, WS_STAT + 128 * P.p * 8); };
    fetch();
#pragma unroll
    for (int i = 0; i < NR; ++i) bb[i] = LD32(rb1, pr0 * 4, (288 * P.c + 128 * i) * 2);
    upto(18);
#pragma nounroll
    for (unsigned it = 0; it < SPIN_CAP; ++it) { bool ok = true;
#pragma unroll
      for (int i = 0; i < NR; ++i)
#pragma unroll
        for (int t = 0; t < 3; ++t) ok = ok && cs[i][t][1] == tag[1] && cs[i][t][3] == tag[1];
      if (KX) ok = (ok || lane >= 16) && st[1] == tag[1] && st[3] == tag[1];
      if (__builtin_expect(__all(ok), 1)) break;
      __builtin_amdgcn_s_sleep(4); asm volatile("" ::: "memory"); fetch(); }
#pragma unroll
    for (int i = 0; i < NR; ++i) { float c0 = __uint_as_float(cs[i][0][0]) + __uint_as_float(cs[i][1][0]) + __uint_as_float(cs[i][2][0]), c1 = __uint_as_float(cs[i][0][2]) + __uint_as_float(cs[i][1][2]) + __uint_as_float(cs[i][2][2]);
      float h0 = rbf(c0), h1 = rbf(c1); u32x4 e = {pk_bf(h0, c0 - h0), bb[i] & 0xffffu, pk_bf(h1, c1 - h1), bb[i] >> 16};
      if (KX) { if (lane >= 16) e = (u32x4){0, 0, 0, 0}; *(LAS u32x4*)(size_t)(LMISC + (lane < 16 ? 1024 + pr0 * 16 : 3840)) = e; }
      else *(LAS u32x4*)(size_t)(LMISC + 1024 + (lane + 64 * i) * 16) = e; }
    if (KX) { *(LAS u32x4*)(size_t)(LMISC + lane * 16) = (u32x4){(st[0] & 0xffffu) | (st[0] << 16), st[0] >> 16, (st[2] & 0xffffu) | (st[2] << 16), st[2] >> 16};
      *(LAS f32x2*)(size_t)(LMISC + 3328 + lane * 8) = (f32x2){__builtin_amdgcn_rcpf(bf_hi(st[0])), __builtin_amdgcn_rcpf(bf_hi(st[2]))}; }
  }
  WAITV(0);
  upto(20);
  u32x4 dd[18];
  if (KX && GPOL) { upto(21); gen_ops(dd, 512 + 16 * P.nt, lane_id(), [&](int) {}); }
  if (KX == 0) {
    unsigned tk;
    auto arrive = [&](int which) { tk = 1; asm volatile("buffer_atomic_add %0, %1, %2, %3 offen sc0" : "+v"(tk) : "v"(lane ? OOB : 0u), "s"(rwd), "s"(WS_F2 + which * 2048 + P.p * 128) : "memory"); };
    auto last = [&](int which) { WAITV(0); PIN(tk); ST64(tag, rws, (lane == 0 && tk == tag[0] + 15u) ? 0 : OOB, WS_F2 + 4096 + which * 2048 + P.p * 128); };
    arrive(0); last(0);
    upto(21);
    if (GPOL) gen_ops(dd, 512 + 16 * P.nt, lane_id(), [&](int) {});
    upto(22);
    ST64(tag, rws, lane ? OOB : 0, WS_F1 + (P.c * 16 + P.p) * 128);
    Flags<1> fe, fw; fe.r = fw.r = rws; fe.tag = fw.tag = tag; fe.S = fw.S = 0;
    fw.vo = (mid && lane < 16) ? WS_F2 + 4096 + lane * 128 : OOB;
    fe.vo = !mid ? OOB : WS_F1 + (P.ub4 < 32 ? P.ub4 * 8 + (lane & 31) : (lane >> 4) * 64 + (lane & 15)) * 128;
    upto(23); fw.look();
    upto(20 + KM - 4); fe.look();
    upto(20 + KM - 3); fw.template await<8>(); fe.template await<8>();
    code_pf(lane, LWL + 8192);
    upto(20 + KM + 5);
    arrive(1); upto(T + 1); last(1);
  }
  if (GPOL) upto(37); else { upto(38); __builtin_amdgcn_s_setprio(0); gen_ops(dd, 512 + 16 * P.nt, lane_id(), [&](int) {}); }
  const unsigned xo = (P.j + 64 * (16 * KX + (lane & 15))) * 1152 + 8 * (lane >> 4) + 1024 + P.nt * 32;
  const u32x4 hi = LD128(mk_rs(TABP, ct::TAB_BYTES), lane * 16, ct::OFF_HS + 2048 + KX * 1024);
  const u32x2 xv = LD64(mk_rs(A.x, 2048 * 1152), xo, 0), bb2 = LD64(mk_rs(A.b2, 1152), 8 * (lane >> 4), 1024 + P.nt * 32);
  upto(40);
  u32x2 pl[16];
#pragma unroll
  for (int i = 0; i < 16; ++i) pl[i] = LD64C(rws, lane * 16 + KX * 8, WS_PL + (((P.p * 4 + P.fq) * 4 + P.nt) * 16 + i) * 1024);
  asm volatile("" ::: "memory");
  const f32x4 y = mfma_bfu(idft(dd, lane, [](int) {}), hi, (f32x4){0, 0, 0, 0});
  f32x4 s = un4(pl[0]);
#pragma unroll
  for (int i = 1; i < 16; ++i) s += un4(pl[i]);
  __builtin_amdgcn_raw_buffer_store_b64(gate(y, s, *(LAS float*)(size_t)(LMISC + 3328 + (32 * P.fq + 16 * KX + (lane & 15)) * 4), xv, bb2), mk_rs(A.out, 2048 * 1152), xo, 0, 0);
}

#define _st stv
template <bool X, bool EX> DI void phaseA(const Args& A, const int w, int& stv, Tr& tr) {
  const int lane = lane_id();
  STAMP(0);
  const Pos P = where();
  int g = lane >> 4, n = lane & 15; const int mh = (w >> 1) & 1, nh = w & 1;
  const u32x4 rwd = mk_rsrc(A.ws, WS_END);
  unsigned l16 = lane * 16;
  constexpr bool DFT = !X || EX;
  const int u0 = X ? 4 * nh : 10 + 4 * nh;
  const int mx = (X && EX) ? 2 * nh : 0;
  unsigned rdA[2][2], tb[2][4];
  { const unsigned a = LA + (64 * mh + n) * 128 + ((g ^ ((n >> 1) & 7)) << 4), trl = LB + (8 * g + (n >> 2)) * 576 + 8 * (n & 3);
    rdA[0][0] = a + mx * 2048; rdA[0][1] = a - mx * 2048; rdA[1][0] = rdA[0][0] ^ 64; rdA[1][1] = rdA[0][1] ^ 64;
    tb[0][0] = trl + u0 * 32 + 32 * (g & 1); tb[0][1] = trl + u0 * 32 - 32 * (g & 1);
    tb[0][2] = X ? trl + ((8 + nh) ^ (g & 1)) * 32 : LB + 18432 + nh * 1024 + l16; tb[0][3] = LB + 20480 + l16;
#pragma unroll
    for (int k = 0; k < (X && EX ? 4 : 3); ++k) { tb[1][k] = tb[0][k] + 2 * LBS; PIN(tb[0][k]); PIN(tb[1][k]); }
    PIN(rdA[0][0]); PIN(rdA[1][0]);
    if (X && EX) { PIN(rdA[0][1]); PIN(rdA[1][1]); } else { rdA[0][1] = rdA[0][0]; rdA[1][1] = rdA[1][0]; } }
  f32x4 acc[4][5], acce[2];
#pragma unroll
  for (int m = 0; m < 4; ++m)
#pragma unroll
    for (int u = 0; u < 5; ++u) acc[m][u] = (f32x4){0, 0, 0, 0};
  acce[0] = acce[1] = (f32x4){0, 0, 0, 0};
  bf16x8 fa[4], fb[5], fe;
  auto rd = [&](int st) { const int sl = st % 5, h = sl >= 2; const unsigned so = (sl - 2 * h) * LBS, ao = ((st >> 1) % 3) * 16384;
    fa[0] = lds_b128(rdA[st & 1][0] + ao); fa[1] = lds_b128(rdA[st & 1][0] + ao + 2048); fa[2] = lds_b128(rdA[st & 1][1] + ao + 4096); fa[3] = lds_b128(rdA[st & 1][1] + ao + 6144);
#pragma unroll
    for (int u = 0; u < 4; ++u) { unsigned bb = tb[h][u & 1] + so + u * 32; fb[u] = lds_tr(bb, bb + 4 * 576); }
    if (X) fb[4] = lds_tr(tb[h][2] + so, tb[h][2] + so + 4 * 576); else fb[4] = lds_b128(tb[h][2] + so);
    if (X && EX) fe = lds_b128(tb[h][3] + so);
    SCHED(); };
  auto mm = [&]() {
#pragma unroll
    for (int u = 0; u < 5; ++u)
#pragma unroll
      for (int m = 0; m < 4; ++m) acc[m][u] = (X || u < 4) ? mfma_bf(fb[u], fa[m], acc[m][u]) : mfma_bf(fa[m], fb[u], acc[m][u]);
    if (X && EX) { acce[0] = mfma_bf(fa[0], fe, acce[0]); acce[1] = mfma_bf(fa[1], fe, acce[1]); }
    SCHED(); };
  const int dunit = X ? 32 + P.fq : 2 * P.c + nh;
  u32x4 hf[2]; u32x2 csu;
  BAR();
  STAMP(1);
  if (X) rd(0);
  static_for<18>([&](auto I) {
    constexpr int i = I;
    BARRIER();
    if (i) {
#pragma unroll
      for (int m = 0; m < 4; ++m) asm volatile("" : "+v"(acc[m][0]), "+v"(acc[m][1]), "+v"(acc[m][2]), "+v"(acc[m][3]), "+v"(acc[m][4]));
      if (X && EX) asm volatile("" : "+v"(acce[0]), "+v"(acce[1])); }
    SCHED();
    if (!X && (i == 6 || i == 8 || i == 10) && (w & 3) == (i - 6) / 2) {
      const unsigned w0 = (1152 * P.fq + ((P.xcd >> 2) * 32 + P.idx) * 18) * 1152;
      dma4(LJUNK, lane_id() * 128, mk_rsrc(A.w2, w0 + 18 * 1152), w0 + (w & 3) * 8192); }
    if (!X && i == 12 && w == 7) dma4(LJUNK, lane_id() * 128, mk_rsrc(TABP, ct::OFF_MQ + P.xcd * 65536 + P.idx * 2048 + 2048), ct::OFF_MQ + P.xcd * 65536 + P.idx * 2048);
    if (!X && i == 16) WAITV(0);
    if (DFT && i == 16) { const rsrc_t rt = mk_rs(TABP, ct::TAB_BYTES); const int le = lane_id(); hf[0] = LD128(rt, le * 16, ct::OFF_HS); hf[1] = LD128(rt, le * 16, ct::OFF_HS + 1024); csu = LD64(rt, le >= 16 ? OOB : le * 8, ct::OFF_HS + 4096 + dunit * 128); }
    if (X) { mm(); if (i < 17) rd(i + 1); else BARRIER(); } else { rd(i); if (i == 17) { BARRIER(); if (YP & 1) __builtin_amdgcn_s_setprio(1); SCHED(); } mm(); }
  });
  STAMP(3);
  { const int le = lane_id(); g = le >> 4; n = le & 15; l16 = le * 16; }
  { u32x2 xs[4], fs[5];
#pragma unroll
    for (int m = 0; m < 4; ++m) xs[m] = lds_u2(LMISC + (64 * mh + 16 * (m ^ mx) + n) * 8);
    const unsigned fo = g ? LMISC + 3840 : LMISC + 1024 + (u0 * 16 + n) * 8, fst = g ? 0 : 128;
#pragma unroll
    for (int u = 0; u < 4; ++u) fs[u] = lds_u2(fo + u * fst);
    if (X) fs[4] = lds_u2(g ? LMISC + 3840 : LMISC + 1024 + ((8 + nh) * 16 + n) * 8);
    if (DFT) {
      if (X) { acce[0] = mfma16(xs[0], csu, acce[0]); acce[1] = mfma16(xs[1], csu, acce[1]); }
      else {
#pragma unroll
        for (int m = 0; m < 4; ++m) acc[m][4] = mfma16(xs[m], csu, acc[m][4]); } }
#pragma unroll
    for (int m = 0; m < 4; ++m)
#pragma unroll
      for (int u = 0; u < (X ? 5 : 4); ++u) acc[m][u] = mfma16(fs[u], xs[m], acc[m][u]);
  }
  auto dft_part = [&]() {
  if (DFT) {
    auto dft_in = [&](f32x4 t0, f32x4 t1, int row0) {
      unsigned sa = LMISC + 3328 + (64 * mh + row0 + 4 * g) * 4;
      t0 *= lds_f4(sa); t1 *= lds_f4(sa + 64);
      return (u32x4){pk_h(t0[0], t0[1]), pk_h(t0[2], t0[3]), pk_h(t1[0], t1[1]), pk_h(t1[2], t1[3])}; };
    auto hmul = [&](u32x4 pk, int r2) { f32x4 e = mfma_hu(pk, hf[r2], (f32x4){0, 0, 0, 0}); return (u32x2){pk_h(e[0], e[1]), pk_h(e[2], e[3])}; };
    auto put = [&](u32x4 pk, int jj) { u32x2 e0 = hmul(pk, 0), e1 = hmul(pk, 1);
      auto s0 = __builtin_amdgcn_permlane16_swap(e0[0], e1[0], false, false), s1 = __builtin_amdgcn_permlane16_swap(e0[1], e1[1], false, false);
      ST128(((u32x4){s0[0], s1[0], s0[1], s1[1]}), rws, n * (36 * 4096) + (g & 1) * 32 + (g >> 1) * 16, WS_E + (dunit * 64 + 4 * P.p + 2 * mh + jj) * 64); };
    if (!X) { u32x4 p0 = dft_in(acc[0][4], acc[1][4], 0), p1 = dft_in(acc[2][4], acc[3][4], 32); put(p0, 0); put(p1, 1); }
    else put(dft_in(acce[0], acce[1], 32 * nh), nh);
  }
  };
  if (!X) dft_part();
  { const int kb0 = u0 >> 1;
#pragma unroll
    for (int m = 0; m < 4; ++m) {
      const int ts = 4 * mh + (m ^ mx);
      const unsigned lo = LH + ts * 1024 + l16, so = WS_H + ((8 * P.p + ts) * 144 + 9 * P.c + kb0) * 1024;
#pragma unroll
      for (int k = 0; k < 2; ++k) { u32x2 a = relu2(acc[m][2 * k]), c2 = relu2(acc[m][2 * k + 1]); u32x4 v = {a[0], a[1], c2[0], c2[1]};
        ST128(v, rws, l16, so + k * 1024); *(LAS u32x4*)(size_t)(lo + (kb0 + k) * 8192) = v; }
      if (X) *(LAS u32x2*)(size_t)(lo + 4 * 8192 + nh * 8) = relu2(acc[m][4]);
    } }
  if (X) dft_part();
  if (!X && (YP & 1)) __builtin_amdgcn_s_setprio(0);
  BARRIER();
  ST128(*(LAS u32x4*)(size_t)(LH + 4 * 8192 + w * 1024 + l16), rws, l16, WS_H + ((8 * P.p + w) * 144 + 9 * P.c + 4) * 1024);
  STAMP(4);
}

template <bool X, bool MID> DI void phaseB(const Args& A, const int w, int& stv, const u32x2 tag, Tr& tr) {
  const Pos P = where();
  const int p = P.p, fq = P.fq, nt = P.nt, q = P.q, j = P.j;
  const int mh = (w >> 1) & 1, nh = w & 1, mq = w & 3;
  constexpr int cl = X ? 0 : 1;
  const int ub = P.ub4 + 2 * cl;
  const u32x4 rwd = mk_rsrc(A.ws, WS_END);
  const rsrc_t rws = mk_rs(A.ws, WS_END), rt = mk_rs(TABP, ct::TAB_BYTES);
  const unsigned wl = w * 1024;
  int ln = lane_id();
  int g2 = ln >> 4, n2 = ln & 15; const int trw = 4 * g2 + (n2 >> 2);
  unsigned rdH[2], rdW[4], l16 = ln * 16;
  rdH[0] = LH + mh * 4096 + l16; rdH[1] = rdH[0] + 32768; PIN(rdH[0]); PIN(rdH[1]);
#pragma unroll
  for (int u = 0; u < 4; ++u) { rdW[u] = LW + trw * 256 + (((4 * nh + u) ^ (trw & 7)) << 5) + 8 * (n2 & 3); PIN(rdW[u]); }
  f32x4 ac[4][4], al[2][2];
#pragma unroll
  for (int u = 0; u < 16; ++u) ac[u >> 2][u & 3] = (f32x4){0, 0, 0, 0};
  al[0][0] = al[0][1] = al[1][0] = al[1][1] = (f32x4){0, 0, 0, 0};
  bf16x8 fa[4], fb[4];
  auto rd = [&](int s) { const int hs = s % 9, hb = hs >= 4; const unsigned ho = (hs - 4 * hb) * 8192, wo = (s & 7) * 8192;
#pragma unroll
    for (int m = 0; m < 4; ++m) fa[m] = lds_b128(rdH[hb] + ho + m * 1024);
#pragma unroll
    for (int u = 0; u < 4; ++u) fb[u] = lds_tr(rdW[u] + wo, rdW[u] + wo + 4096);
    SCHED(); };
  auto mm = [&]() {
#pragma unroll
    for (int u = 0; u < 4; ++u)
#pragma unroll
      for (int m = 0; m < 4; ++m) ac[m][u] = mfma_bf(fb[u], fa[m], ac[m][u]);
    SCHED(); };
  unsigned rdL0 = LWL + trw * 128 + (((2 * cl) ^ ((trw >> 1) & 3)) << 5) + 8 * (n2 & 3), rdL1 = rdL0 ^ 32, rdHL = LH + mq * 2048 + l16 + 8192;
  PIN(rdL0); PIN(rdL1); PIN(rdHL);
  auto stepL = [&](int s) { const unsigned l = (s < 7 ? s : s - 5) * 4096, ha = s ? rdHL + (s - 1) * 8192 : rdHL - 8192;
    bf16x8 a0 = lds_b128(ha), a1 = lds_b128(ha + 1024), b0 = lds_tr(rdL0 + l, rdL0 + l + 2048), b1 = lds_tr(rdL1 + l, rdL1 + l + 2048);
    al[0][0] = mfma_bf(b0, a0, al[0][0]); al[1][0] = mfma_bf(b0, a1, al[1][0]); al[0][1] = mfma_bf(b1, a0, al[0][1]); al[1][1] = mfma_bf(b1, a1, al[1][1]); };
  u32x4 mt4[4], wq[2], gq[2]; unsigned sx[4] = {0, 0, 0, 0};
  const unsigned wso = WS_WF + q * (19 * 8192) + mq * 1024, tq = LE + cl * 8192;
  if (X) rd(0);
  static_for<18>([&](auto K) {
    constexpr int k = K;
    if (k) {
      if (k == 1 || k == KM + 4) WAITV(0);
      BARRIER();
#pragma unroll
      for (int m = 0; m < 4; ++m) asm volatile("" : "+v"(ac[m][0]), "+v"(ac[m][1]), "+v"(ac[m][2]), "+v"(ac[m][3]));
      if (k <= 4 - LPOL) asm volatile("" : "+v"(al[0][0]), "+v"(al[0][1]), "+v"(al[1][0]), "+v"(al[1][1]));
    }
    SCHED();
    auto side = [&]() {
      const int ln = (k == 2 || k == 4 - LPOL || k == KM - 2 || k == KM + 2) ? lane_id() : 0, g2 = ln >> 4, n2 = ln & 15; const unsigned l16 = ln * 16, wvo = n2 * 64 + g2 * 16;
      if (k == 4 - LPOL) {
#pragma unroll
        for (int u = 0; u < 2; ++u) ST128(pk8(al[0][u], al[1][u]), rws, l16, WS_PL + (((p * 4 + mq) * 4 + 2 * cl + u) * 16 + P.c) * 1024); }
      if (k == 2 && MID) {
#pragma unroll
        for (int i = 0; i < 4; ++i) mt4[i] = LD128(rt, l16, ct::OFF_MQ + q * 32768 + (mq * 4 + i) * 1024); }
      if (k == KM - 2 && MID) { wq[0] = LD128C(rws, wvo, wso + (ub >> 1) * 8192); wq[1] = LD128C(rws, wvo, wso + (ub >> 1) * 8192 + 4096); }
      if (X && k == KM - 2 && MID) { const rsrc_t r0 = mk_rs(A.ws, ub ? 0 : WS_END);
        const u32x3 s0 = __builtin_amdgcn_raw_buffer_load_b96(r0, wvo, wso + 18 * 8192 + 4, 16), s1 = __builtin_amdgcn_raw_buffer_load_b96(r0, wvo, wso + 18 * 8192 + 4096 + 4, 16);
        sx[0] = s0[0]; sx[1] = s1[0]; sx[2] = s0[2]; sx[3] = s1[2]; }
      if (k == KM + 2 && MID) { const unsigned so = WS_G + (q * 64 + 16 * mq) * 1152 + ub * 32;
        ST128(gq[0], rws, n2 * 1152 + g2 * 16, so); ST128(gq[1], rws, n2 * 1152 + g2 * 16, so + 16 * 64 * 1152); }
      asm volatile("" ::: "memory"); SCHED(); };
    auto midw = [&]() {
      const int ln = lane_id(), g2 = ln >> 4, n2 = ln & 15, trw = 4 * g2 + (n2 >> 2); const unsigned l16 = ln * 16;
      if (k == KM) {
        const unsigned tra = tq + trw * 64 + ((n2 >> 1) & 1) * 4096 + (n2 & 1) * 16, trc = tra + ((q == 0 && mq >= 2) ? 32 : 0);
        const u32x2 wf[4] = {wq[0].xy, wq[1].xy, wq[0].zw, wq[1].zw};
        const float z = q ? 1.f : 0.f;
        const float al_ = (q == 0 && mq == 0 && n2 == 0) ? 0.f : 1.f;
#pragma unroll
        for (int v = 0; v < 2; ++v) { f32x4 p1 = {0, 0, 0, 0}, p2 = p1, p3 = p1, p4 = p1;
#pragma unroll
          for (int jk = 0; jk < 2; ++jk) { const unsigned o = v * 8 + jk * 2048; u32x4 ec = lds_tru(trc + o, trc + o + 1024), es = lds_tru(tra + o + 32, tra + o + 32 + 1024);
            p1 = mfma_hu(ec, mt4[jk], p1); p2 = mfma_hu(es, mt4[2 + jk], p2); p3 = mfma_hu(ec, mt4[2 + jk], p3); p4 = mfma_hu(es, mt4[jk], p4); }
          asm volatile("" : "+v"(p1), "+v"(p2), "+v"(p3), "+v"(p4) :: "memory"); SCHED();
          if (v) {
#pragma unroll
            for (int i = 0; i < 4; ++i) mt4[i] = LD128(rt, l16, ct::OFF_MQ + q * 32768 + 16384 + (mq * 4 + i) * 1024);
            asm volatile("" ::: "memory"); SCHED(); }
          const f32x4 a = p1 - z * p2, bb = p3 + z * p4; f32x4 ao, bo;
#pragma unroll
          for (int e = 0; e < 2; ++e) {
            auto ex = [&](unsigned dd) { return e ? bf_hi(dd) : bf_lo(dd); };
            f32x2 wp = {ex(wf[0][v]), ex(wf[2][v])}, wm = {ex(wf[1][v]), ex(wf[3][v])}, S = wp + al_ * wm, Tt = al_ * wp + wm, U = al_ * (wp - wm);
            f32x2 A2 = {a[2 * e], a[2 * e + 1]}, B2 = {bb[2 * e], bb[2 * e + 1]}, iA = {-A2[1], A2[0]}, iB = {-B2[1], B2[0]};
            f32x2 o1 = A2 * S[0] + iA * S[1] - iB * U[0] + B2 * U[1], o2 = B2 * Tt[0] + iB * Tt[1] + iA * U[0] - A2 * U[1];
            if (X && v == 0 && e == 0) {
              const f32x2 S2 = {S[0], bf_lo(sx[0]) + al_ * bf_lo(sx[1])}, T2 = {Tt[0], al_ * bf_lo(sx[0]) + bf_lo(sx[1])}, U2 = {U[1], al_ * (bf_lo(sx[2]) - bf_lo(sx[3]))};
              const f32x2 r1 = 0.5f * (A2 * S2 + B2 * U2), r2 = 0.5f * (B2 * T2 - A2 * U2);
              const bool pc = ub == 0 && g2 == 0; o1 = pc ? r1 : o1; o2 = pc ? r2 : o2; }
            ao[2 * e] = o1[0]; ao[2 * e + 1] = o1[1]; bo[2 * e] = o2[0]; bo[2 * e + 1] = o2[1]; }
          gq[v] = pk8(ao, bo); PIN(gq[v]); SCHED(); } }
      if (k == KM + 1) {
#pragma unroll
        for (int v = 0; v < 2; ++v) { unsigned yo = tq + v * 4096 + mq * 1024 + n2 * 32 + (g2 ^ (n2 >> 2)) * 8;
          *(LAS u32x2*)(size_t)yo = gq[v].xy; *(LAS u32x2*)(size_t)(yo + 512) = gq[v].zw; } }
      if (k == KM + 2) {
        f32x4 gc[2][2], gs[2];
        gc[0][0] = gc[0][1] = gc[1][0] = gc[1][1] = gs[0] = gs[1] = (f32x4){0, 0, 0, 0};
#pragma unroll
        for (int mt = 0; mt < 4; ++mt) { const u32x4 m2 = {mt4[mt][2] ^ 0x80008000u, mt4[mt][3] ^ 0x80008000u, mt4[mt][0], mt4[mt][1]};
#pragma unroll
          for (int v = 0; v < 2; ++v) { const unsigned o = tq + trw * 32 + v * 4096 + mt * 1024 + 8 * ((n2 & 3) ^ g2); u32x4 y = lds_tru(o, o + 512);
            gc[v][mt >> 1] = mfma_bfu(y, mt4[mt], gc[v][mt >> 1]); gs[v] = mfma_bfu(y, m2, gs[v]); }
          if (mt == 1) SCHED(); }
        SCHED();
        const float z = q ? 1.f : 0.f;
        gq[0] = pk8(gc[0][0] + z * gc[0][1], gc[1][0] + z * gc[1][1]); gq[1] = pk8(z * gs[0] + (1.f - z) * gc[0][1], z * gs[1] + (1.f - z) * gc[1][1]); PIN(gq[0]); PIN(gq[1]); }
      SCHED(); };
    auto lwork = [&]() { if (k == 0) { stepL(0); stepL(1); SCHED(); stepL(2); stepL(3); }
      if (LPOL) { if (k == 1) { stepL(4); stepL(5); } if (k == 2) stepL(6); if (k == 3) { stepL(7); stepL(8); } }
      else { if (k == 1) stepL(4); if (k == 2) { stepL(5); stepL(6); } if (k == 3) stepL(7); if (k == 4) stepL(8); }
      SCHED(); };
    auto hold = [&]() {
#pragma unroll
      for (int m = 0; m < 4; ++m) asm volatile("" : "+v"(ac[m][0]), "+v"(ac[m][1]), "+v"(ac[m][2]), "+v"(ac[m][3])); };
    constexpr bool MK = k == KM || k == KM + 2;
    if (X) { mm(); if (MK) { hold(); if (MID) midw(); } lwork(); if (k < 17) rd(2 * k + 2); side(); }
    else { if (MK && MID) midw(); lwork(); rd(2 * k + 1); mm(); side(); }
    if (MID && k == KM + 1) midw();
    if (k == 0) STAMP(5);
    if (k == 1) STAMP(6);
    if (k == 2) STAMP(7);
    if (k == 4) STAMP(11);
    if (k == 7) STAMP(21);
    if (k == 10) STAMP(10);
    if (k == 14) STAMP(19);
  });
  STAMP(12);
  if (!X && (YP & 2)) __builtin_amdgcn_s_setprio(1);
  { const int le = lane_id(); ln = le; g2 = le >> 4; n2 = le & 15; l16 = le * 16; }
  const unsigned xo = (j + 64 * n2) * 1152 + 8 * g2 + nt * 256 + w * 32;
  const int jk = 2 * mh + cl;
  { const unsigned xs = xslot(w) + l16;
#pragma unroll
    for (int u = 0; u < 4; ++u) { *(LAS f32x4*)(size_t)(xs + u * 2048) = ac[2 - 2 * cl][u]; *(LAS f32x4*)(size_t)(xs + u * 2048 + 1024) = ac[3 - 2 * cl][u]; }
    if (jk == fq) { const unsigned xk = xslot(8 + nh) + l16;
#pragma unroll
      for (int u = 0; u < 4; ++u) { *(LAS f32x4*)(size_t)(xk + u * 2048) = ac[2 * cl][u]; *(LAS f32x4*)(size_t)(xk + u * 2048 + 1024) = ac[2 * cl + 1][u]; } } }
  u32x4 hi[2]; u32x2 xv[2], bb2;
  auto small = [&]() { asm volatile("" ::: "memory"); SCHED();
    hi[0] = LD128(rt, l16, ct::OFF_HS + 2048); hi[1] = LD128(rt, l16, ct::OFF_HS + 3072);
    const rsrc_t rx = mk_rs(A.x, 2048 * 1152);
    xv[0] = LD64(rx, xo, 0); xv[1] = LD64(rx, xo, 16 * 64 * 1152); bb2 = LD64(mk_rs(A.b2, 1152), 8 * g2, nt * 256 + w * 32);
    asm volatile("" ::: "memory"); };
  if (X) small();
  BARRIER(); SCHED();
  if (jk != fq) { const unsigned po = WS_P + ((((p * 4 + nt) * 4 + jk) * 4 + fq) * 8 + 4 * nh) * 1024, xr2 = xslot(w ^ 4) + l16;
    u32x4 pv[4];
#pragma unroll
    for (int u = 0; u < 4; ++u) pv[u] = pk8(ac[2 * cl][u] + lds_f4(xr2 + u * 2048), ac[2 * cl + 1][u] + lds_f4(xr2 + u * 2048 + 1024));
#pragma unroll
    for (int u = 0; u < 4; ++u) ST128(pv[u], rws, l16, po + u * 1024);
  }
  u32x4 dd[18], pp[3];
  gen_ops(dd, 128 * nt + 16 * w, ln, [&](int i) { if (i == FLAGI) { WAITV(0);
    ST64(tag, rws, ln ? OOB : 0, jk != fq ? WS_FP + ((((p * 4 + nt) * 4 + jk) * 2 + nh) * 4 + fq) * 128 : OOB);
    if (!X) small(); } });
  if (!X && (YP & 2)) __builtin_amdgcn_s_setprio(0);
  STAMP(13);
  BARRIER();
  STAMP(20);
  Flags<1> fp; fp.r = rws; fp.tag = tag; fp.S = 0; fp.vo = (ln < 4 && ln != fq) ? WS_FP + ((((p * 4 + nt) * 4 + fq) * 2 + cl) * 4 + ln) * 128 : OOB;
  fp.look();
  const u32x4 pk = idft(dd, ln, [&](int t) { if (t == 1) { fp.template await<2>();
#pragma unroll
    for (int i = 0; i < 3; ++i) pp[i] = LD128C(rws, l16, WS_P + (((p * 4 + nt) * 4 + fq) * 4 + ((fq + 1 + i) & 3)) * 8192 + wl);
    asm volatile("" ::: "memory"); } });
  STAMP(14);
  const unsigned a = xslot(((fq >> 1) * 2 + cl) + (1 - (fq & 1)) * 4) + l16 + mq * 2048, a2 = xslot(8 + cl) + l16 + mq * 2048;
  const rsrc_t rout = mk_rs(A.out, 2048 * 1152);
#pragma unroll
  for (int r2 = 0; r2 < 2; ++r2) {
    f32x4 sm = lds_f4(a + r2 * 1024) + lds_f4(a2 + r2 * 1024);
#pragma unroll
    for (int i = 0; i < 3; ++i) sm += un4(r2 ? pp[i].zw : pp[i].xy);
    __builtin_amdgcn_raw_buffer_store_b64(gate(mfma_bfu(pk, hi[r2], (f32x4){0, 0, 0, 0}), sm, *(LAS float*)(size_t)(LMISC + 3328 + (32 * fq + 16 * r2 + n2) * 4), xv[r2], bb2), rout, xo, r2 * (16 * 64 * 1152), 0); }
  STAMP(18); STAMP_FLUSH();
}

__global__ __launch_bounds__(768, 3) void fused(Args A) {
  const int w = __builtin_amdgcn_readfirstlane(threadIdx.x >> 6);
  const bool ex = ((blockIdx.x >> 3) & 3) == 0, mid = !ex && (blockIdx.x >> 5) < 6;
  Tr tr{};
  if (__builtin_expect((w >> 1) == 4, 1)) loader(A, w - 8, mid, tr);
  else if (w >= 10) {
    if (w == 10) { code_pf(lane_id(), LJUNK); worker<0>(A, mid, tr); } else worker<1>(A, mid, tr);
  } else {
    int stv = 0;
    const u32x2 tag0 = get_tag(A.ws);
    if (w < 4) {
      if (ex) phaseA<true, true>(A, w, stv, tr); else phaseA<true, false>(A, w, stv, tr);
      if (mid) phaseB<true, true>(A, w, stv, tag0, tr); else phaseB<true, false>(A, w, stv, tag0, tr);
    } else {
      phaseA<false, false>(A, w, stv, tr);
      if (mid) phaseB<false, true>(A, w, stv, tag0, tr); else phaseB<false, false>(A, w, stv, tag0, tr);
    } }
}
__device__ __attribute__((noinline, used)) void code_end_marker() {}

extern "C" void kernel_launch(void* const* d_in, const int* in_sizes, int n_in, void* d_out, int out_size, void* d_ws, size_t ws_size, hipStream_t stream) {
  Args A;
  A.x = (const u16*)d_in[0]; A.wr = (const u16*)d_in[1]; A.wi = (const u16*)d_in[2]; A.w1 = (const u16*)d_in[4]; A.b1 = (const u16*)d_in[5];
  A.w2 = (const u16*)d_in[6]; A.b2 = (const u16*)d_in[7]; A.out = (u16*)d_out; A.ws = (char*)d_ws;
  (void)hipFuncSetAttribute((const void*)fused, hipFuncAttributeMaxDynamicSharedMemorySize, LDS_BYTES);
  fused<<<256, 768, LDS_BYTES, stream>>>(A);
}
```
